```python
import torch

def _softmax(x, axis=-1):
    return torch.softmax(x, dim=axis)

def _gelu(x):
    return torch.nn.functional.gelu(x, approximate='tanh')

def _silu(x):
    return torch.nn.functional.silu(x)

def _sigmoid(x):
    return torch.sigmoid(x)

def _layernorm(x, axis=-1, eps=1e-05):
    m = x.mean(dim=axis, keepdim=True)
    v = x.var(dim=axis, keepdim=True, unbiased=False)
    return (x - m) / torch.sqrt(v + eps)

def _scatter_add(tgt, idx, src, axis=0):
    return tgt.clone().index_add_(axis, idx, src)

def _segment_sum(src, seg, n):
    out = torch.zeros((n,) + src.shape[1:], dtype=src.dtype)
    return out.index_add_(0, seg, src)

def _pad(x, pw):
    flat = tuple((v for pair in reversed(pw) for v in pair))
    return torch.nn.functional.pad(x, flat)
kernel_name = 'standalone__varlen_attn_packed__ragged'

def setup_inputs():

    _w = lambda a: (lambda t: t.to(torch.bfloat16) if t.is_floating_point() else t)(torch.as_tensor(a))
    cu = torch.tensor([0, 321, 555, 878, 1299, 1551, 1670, 1878, 2181, 2351, 2646, 2810, 3248, 3471, 3811, 4169, 4391], dtype=torch.int64)
    T, H, D = (int(cu[-1]), 16, 128)
    return {'q': _w((lambda *s: torch.randn(*s, dtype=torch.bfloat16) * 0.8)(T, H, D)), 'k': _w((lambda *s: torch.randn(*s, dtype=torch.bfloat16) * 0.8)(T, H, D)), 'v': _w((lambda *s: torch.randn(*s, dtype=torch.bfloat16) * 0.05)(T, H, D)), 'cu': _w(cu)}

def reference(q, k, v, cu):
    T, H, D = q.shape
    out = torch.zeros_like(q)
    scale = D ** (-0.5)
    outs = []
    for i in range(len(cu) - 1):
        s, e = (int(cu[i]), int(cu[i + 1]))
        qi, ki, vi = (q[s:e].clone(), k[s:e].clone(), v[s:e].clone())
        p = _softmax(torch.einsum('qhd,khd->hqk', qi, ki) * scale, axis=-1)
        outs.append(torch.einsum('hqk,khd->qhd', p, vi))
    return torch.concatenate(outs, axis=0)
```

```cpp
#include <hip/hip_runtime.h>
#include <hip/hip_bf16.h>

typedef float    f32x16 __attribute__((ext_vector_type(16)));
typedef short    bf16x8 __attribute__((ext_vector_type(8)));
typedef short    s16x4  __attribute__((ext_vector_type(4)));
typedef unsigned u32x4  __attribute__((ext_vector_type(4)));
typedef unsigned u32x2  __attribute__((ext_vector_type(2)));
typedef __attribute__((address_space(3))) const char* lds_cptr;
#define DI __device__ __forceinline__

constexpr int H = 16, D = 128;
constexpr unsigned ROWB = H * D * 2;
constexpr int NS = 3;
constexpr float C2 = 0.08838834764831845f * 1.4426950408889634f;
constexpr float THR = 8.0f;

DI unsigned rfl(unsigned x) { return __builtin_amdgcn_readfirstlane(x); }
DI unsigned cvtpk(float lo, float hi) { unsigned r; asm("v_cvt_pk_bf16_f32 %0, %1, %2" : "=v"(r) : "v"(lo), "v"(hi)); return r; }
DI void dma16(unsigned m0v, unsigned voff, __amdgpu_buffer_rsrc_t srd, unsigned soff) {
  asm volatile("s_mov_b32 m0, %0\n\ts_nop 0\n\tbuffer_load_dwordx4 %1, %2, %3 offen lds" :: "s"(m0v), "v"(voff), "s"(srd), "s"(soff) : "memory");
}
DI s16x4 vtr(lds_cptr p) {
  typedef short v4i16_t __attribute__((ext_vector_type(4)));
  return __builtin_bit_cast(s16x4, __builtin_amdgcn_ds_read_tr16_b64_v4i16((__attribute__((address_space(3))) v4i16_t*)p));
}
#define MFMA(a, b, c) __builtin_amdgcn_mfma_f32_32x32x16_bf16(a, b, c, 0, 0, 0)
#define MX3(a, b, c) __builtin_fmaxf(__builtin_fmaxf((a), (b)), (c))

__global__ __launch_bounds__(256, 3) void attn_v0(const unsigned short* __restrict__ Q, const unsigned short* __restrict__ K,
                                                  const unsigned short* __restrict__ V, const int* __restrict__ cu,
                                                  unsigned short* __restrict__ O, int nseq) {
  __shared__ __attribute__((aligned(16))) char lds[NS * 16384];
  const unsigned tid = threadIdx.x, lane = tid & 63u, l32 = lane & 31u, hi = lane >> 5;
  const unsigned wid = rfl(tid >> 6);
  const unsigned xcd = blockIdx.x & 7u, j = blockIdx.x >> 3, hsel = j & 1u;
  int item = (int)(j >> 1);
  int s0 = -1, seqlen = 0, qb = 0, sidx = 0;
  {
    for (int base = 0; base < nseq; base += 64) {
      const int sl = base + (int)lane;
      int a = 0, L = 0;
      if (sl < nseq) { a = cu[sl]; L = cu[sl + 1] - a; }
      const int nb = (L + 127) >> 7;
      int pre = 0, tot = 0;
      const int cnt = min(64, nseq - base);
      for (int i = 0; i < cnt; ++i) {
        const int Li = __builtin_amdgcn_readlane(L, i), nbi = (Li + 127) >> 7;
        if (Li > L || (Li == L && i < (int)lane)) pre += nbi;
        tot += nbi;
      }
      if (item < tot) {
        const unsigned long long m = __ballot(item >= pre && item < pre + nb);
        const int src = __builtin_ctzll(m);
        s0 = __builtin_amdgcn_readlane(a, src); seqlen = __builtin_amdgcn_readlane(L, src);
        qb = item - __builtin_amdgcn_readlane(pre, src); sidx = base + src;
        break;
      }
      item -= tot;
    }
  }
  if (s0 < 0) return;
  const unsigned h = (2u * xcd + hsel + 2u * (unsigned)sidx) & 15u;
  const size_t seqoff = (size_t)s0 * ROWB + h * 256u;
  const int nrec = (seqlen - 1) * (int)ROWB + 256;
  const __amdgpu_buffer_rsrc_t srdQ = __builtin_amdgcn_make_buffer_rsrc((void*)((const char*)Q + seqoff), (short)0, nrec, 0x00020000);
  const __amdgpu_buffer_rsrc_t srdK = __builtin_amdgcn_make_buffer_rsrc((void*)((const char*)K + seqoff), (short)0, nrec, 0x00020000);
  const __amdgpu_buffer_rsrc_t srdV = __builtin_amdgcn_make_buffer_rsrc((void*)((const char*)V + seqoff), (short)0, nrec, 0x00020000);
  const __amdgpu_buffer_rsrc_t srdO = __builtin_amdgcn_make_buffer_rsrc((void*)((char*)O + seqoff), (short)0, nrec, 0x00020000);
  const int NT = (seqlen + 31) >> 5;
  const int qrow0 = qb * 128 + (int)wid * 32;
  const bool active = qrow0 < seqlen;

  const unsigned ldsb = (unsigned)(size_t)lds;
  const unsigned drow = 8u * wid + ((lane >> 2) & 7u);
  const unsigned vK = drow * ROWB + 64u * hi + 16u * ((lane & 3u) ^ (2u * (wid & 1u) + ((lane >> 4) & 1u)));
  const unsigned vV = drow * ROWB + 64u * hi + 16u * (lane & 3u);
  auto issue = [&](int t, int slot) {
    const unsigned m = rfl(ldsb + (unsigned)slot * 16384u + wid * 2048u), so = rfl((unsigned)t * 32u * ROWB);
    dma16(m, vK, srdK, so); dma16(m + 1024u, vK, srdK, so + 128u);
    dma16(m + 8192u, vV, srdV, so); dma16(m + 8192u + 1024u, vV, srdV, so + 128u);
  };
  bf16x8 qf[8];
  {
    const unsigned vq = (unsigned)(qrow0 + (int)l32) * ROWB + 16u * hi;
#pragma unroll
    for (int d0 = 0; d0 < 8; ++d0) qf[d0] = __builtin_bit_cast(bf16x8, __builtin_amdgcn_raw_buffer_load_b128(srdQ, vq + 32u * d0, 0, 0));
  }
#pragma unroll
  for (int t = 0; t < NS - 1; ++t) issue(t, t);

  const lds_cptr kb0 = (lds_cptr)lds + 2048u * (l32 >> 3) + 64u * (l32 & 7u) + 16u * ((0u + hi) ^ ((l32 >> 2) & 3u));
  const lds_cptr kb1 = (lds_cptr)lds + 2048u * (l32 >> 3) + 64u * (l32 & 7u) + 16u * ((2u + hi) ^ ((l32 >> 2) & 3u));
  const lds_cptr vb  = (lds_cptr)lds + 8192u + 256u * hi + 64u * ((lane >> 2) & 3u) + 32u * ((lane >> 4) & 1u) + 8u * (lane & 3u);

  f32x16 o[4];
#pragma unroll
  for (int i = 0; i < 4; ++i) o[i] = f32x16{};
  float mhat = -1e30f, lsum = 0.f;
  int slot = 0;
  for (int t = 0; t < NT; ++t) {
    asm volatile("s_waitcnt vmcnt(%0)\n\ts_barrier" :: "i"(4 * (NS - 2)) : "memory");
    { int ns = slot + NS - 1; if (ns >= NS) ns -= NS; issue(t + NS - 1, ns); }
    if (active) {
      const unsigned so = (unsigned)slot * 16384u;
      f32x16 s = f32x16{};
#pragma unroll
      for (int d0 = 0; d0 < 8; ++d0) {
        const bf16x8 kf = *(const __attribute__((address_space(3))) bf16x8*)(((d0 & 1) ? kb1 : kb0) + so + 512 * (d0 >> 1));
        s = MFMA(kf, qf[d0], s);
      }
      if (t == NT - 1) {
        const int rem = seqlen - t * 32 - 4 * (int)hi;
#pragma unroll
        for (int r = 0; r < 16; ++r) if ((r & 3) + 8 * (r >> 2) >= rem) s[r] = -INFINITY;
      }
      float mx = MX3(s[0], s[1], s[2]);
#pragma unroll
      for (int r = 3; r < 15; r += 2) mx = MX3(mx, s[r], s[r + 1]);
      mx = __builtin_fmaxf(mx, s[15]);
      { auto rr = __builtin_amdgcn_permlane32_swap(__float_as_uint(mx), __float_as_uint(mx), false, false);
        mx = __builtin_fmaxf(__uint_as_float(rr[0]), __uint_as_float(rr[1])); }
      const float ms = mx * C2;
      if (__builtin_expect(__any(ms - mhat > THR), 0)) {
        const float mn = __builtin_fmaxf(mhat, ms), f = __builtin_amdgcn_exp2f(mhat - mn);
        mhat = mn; lsum *= f;
#pragma unroll
        for (int i = 0; i < 4; ++i)
#pragma unroll
          for (int r = 0; r < 16; ++r) o[i][r] *= f;
      }
      const float nm = -mhat;
      float ps = 0.f;
#pragma unroll
      for (int r = 0; r < 16; ++r) { s[r] = __builtin_amdgcn_exp2f(__builtin_fmaf(s[r], C2, nm)); ps += s[r]; }
      lsum += ps;
      u32x4 p0 = {cvtpk(s[0], s[1]), cvtpk(s[2], s[3]), cvtpk(s[4], s[5]), cvtpk(s[6], s[7])};
      u32x4 p1 = {cvtpk(s[8], s[9]), cvtpk(s[10], s[11]), cvtpk(s[12], s[13]), cvtpk(s[14], s[15])};
      const bf16x8 pf0 = __builtin_bit_cast(bf16x8, p0), pf1 = __builtin_bit_cast(bf16x8, p1);
#pragma unroll
      for (int db = 0; db < 4; ++db) {
        const s16x4 a0 = vtr(vb + so + 512 * db), a1 = vtr(vb + so + 2048 + 512 * db);
        const s16x4 b0 = vtr(vb + so + 4096 + 512 * db), b1 = vtr(vb + so + 6144 + 512 * db);
        o[db] = MFMA(__builtin_shufflevector(a0, a1, 0, 1, 2, 3, 4, 5, 6, 7), pf0, o[db]);
        o[db] = MFMA(__builtin_shufflevector(b0, b1, 0, 1, 2, 3, 4, 5, 6, 7), pf1, o[db]);
      }
    }
    slot = (slot + 1 == NS) ? 0 : slot + 1;
  }
  if (active) {
    { auto rr = __builtin_amdgcn_permlane32_swap(__float_as_uint(lsum), __float_as_uint(lsum), false, false);
      lsum = __uint_as_float(rr[0]) + __uint_as_float(rr[1]); }
    const float il = 1.0f / lsum;
    const unsigned vo = (unsigned)(qrow0 + (int)l32) * ROWB + 16u * hi;
#pragma unroll
    for (int db = 0; db < 4; ++db)
#pragma unroll
      for (int k = 0; k < 4; k += 2) {
        unsigned a0 = cvtpk(o[db][4 * k + 0] * il, o[db][4 * k + 1] * il), a1 = cvtpk(o[db][4 * k + 2] * il, o[db][4 * k + 3] * il);
        unsigned b0 = cvtpk(o[db][4 * k + 4] * il, o[db][4 * k + 5] * il), b1 = cvtpk(o[db][4 * k + 6] * il, o[db][4 * k + 7] * il);
        auto r0 = __builtin_amdgcn_permlane32_swap(a0, b0, false, false);
        auto r1 = __builtin_amdgcn_permlane32_swap(a1, b1, false, false);
        u32x4 w = {r0[0], r1[0], r0[1], r1[1]};
        __builtin_amdgcn_raw_buffer_store_b128(w, srdO, vo + 64u * db + 16u * k, 0, 0);
      }
  }
  asm volatile("s_waitcnt vmcnt(0)" ::: "memory");
}

extern "C" void kernel_launch(void* const* d_in, const int* in_sizes, int n_in,
                              void* d_out, int out_size, void* d_ws, size_t ws_size,
                              hipStream_t stream) {
  const int T = in_sizes[0] / (H * D), nseq = in_sizes[3] - 1;
  const int maxitems = T / 128 + nseq;
  attn_v0<<<maxitems * H, 256, 0, stream>>>((const unsigned short*)d_in[0], (const unsigned short*)d_in[1], (const unsigned short*)d_in[2],
                                             (const int*)d_in[3], (unsigned short*)d_out, nseq);
}
```

```cpp
#define EXPT 1
#include <hip/hip_runtime.h>
#include <hip/hip_bf16.h>

typedef float    f32x16 __attribute__((ext_vector_type(16)));
typedef short    bf16x8 __attribute__((ext_vector_type(8)));
typedef short    s16x4  __attribute__((ext_vector_type(4)));
typedef unsigned u32x4  __attribute__((ext_vector_type(4)));
typedef unsigned u32x2  __attribute__((ext_vector_type(2)));
typedef __attribute__((address_space(3))) const char* lds_cptr;
#define DI __device__ __forceinline__

constexpr int H = 16, D = 128;
constexpr unsigned ROWB = H * D * 2;
constexpr int NS = 3;
constexpr float C2 = 0.08838834764831845f * 1.4426950408889634f;
constexpr float THR = 8.0f;

DI unsigned rfl(unsigned x) { return __builtin_amdgcn_readfirstlane(x); }
DI unsigned cvtpk(float lo, float hi) { unsigned r; asm("v_cvt_pk_bf16_f32 %0, %1, %2" : "=v"(r) : "v"(lo), "v"(hi)); return r; }
DI void dma16(unsigned m0v, unsigned voff, __amdgpu_buffer_rsrc_t srd, unsigned soff) {
  asm volatile("s_mov_b32 m0, %0\n\ts_nop 0\n\tbuffer_load_dwordx4 %1, %2, %3 offen lds" :: "s"(m0v), "v"(voff), "s"(srd), "s"(soff) : "memory");
}
DI s16x4 vtr(lds_cptr p) {
  typedef short v4i16_t __attribute__((ext_vector_type(4)));
  return __builtin_bit_cast(s16x4, __builtin_amdgcn_ds_read_tr16_b64_v4i16((__attribute__((address_space(3))) v4i16_t*)p));
}
#define MFMA(a, b, c) __builtin_amdgcn_mfma_f32_32x32x16_bf16(a, b, c, 0, 0, 0)
#define MX3(a, b, c) __builtin_fmaxf(__builtin_fmaxf((a), (b)), (c))

__global__ __launch_bounds__(256, 3) void attn_v0(const unsigned short* __restrict__ Q, const unsigned short* __restrict__ K,
                                                  const unsigned short* __restrict__ V, const int* __restrict__ cu,
                                                  unsigned short* __restrict__ O, int nseq) {
  const unsigned long long ts0 = __builtin_amdgcn_s_memrealtime(), c0 = __builtin_amdgcn_s_memtime(); unsigned long long ts1 = 0, ts15 = 0;
  __shared__ __attribute__((aligned(16))) char lds[NS * 16384];
  const unsigned tid = threadIdx.x, lane = tid & 63u, l32 = lane & 31u, hi = lane >> 5;
  const unsigned wid = rfl(tid >> 6);
  const unsigned xcd = blockIdx.x & 7u, j = blockIdx.x >> 3, hsel = j & 1u;
  int item = (int)(j >> 1);
  int s0 = -1, seqlen = 0, qb = 0, sidx = 0;
  {
    for (int base = 0; base < nseq; base += 64) {
      const int sl = base + (int)lane;
      int a = 0, L = 0;
      if (sl < nseq) { a = cu[sl]; L = cu[sl + 1] - a; }
      const int nb = (L + 127) >> 7;
      int pre = 0, tot = 0;
      const int cnt = min(64, nseq - base);
      for (int i = 0; i < cnt; ++i) {
        const int Li = __builtin_amdgcn_readlane(L, i), nbi = (Li + 127) >> 7;
        if (Li > L || (Li == L && i < (int)lane)) pre += nbi;
        tot += nbi;
      }
      if (item < tot) {
        const unsigned long long m = __ballot(item >= pre && item < pre + nb);
        const int src = __builtin_ctzll(m);
        s0 = __builtin_amdgcn_readlane(a, src); seqlen = __builtin_amdgcn_readlane(L, src);
        qb = item - __builtin_amdgcn_readlane(pre, src); sidx = base + src;
        break;
      }
      item -= tot;
    }
  }
  if (s0 < 0) { if (EXPT == 6 && blockIdx.x == gridDim.x - 1) { while (__builtin_amdgcn_s_memrealtime() < ts0 + 5000) __builtin_amdgcn_s_sleep(1); } return; }
  ts15 = __builtin_amdgcn_s_memrealtime();
  const unsigned h = (2u * xcd + hsel + 2u * (unsigned)sidx) & 15u;
  const size_t seqoff = (size_t)s0 * ROWB + h * 256u;
  const int nrec = (seqlen - 1) * (int)ROWB + 256;
  const __amdgpu_buffer_rsrc_t srdQ = __builtin_amdgcn_make_buffer_rsrc((void*)((const char*)Q + seqoff), (short)0, nrec, 0x00020000);
  const __amdgpu_buffer_rsrc_t srdK = __builtin_amdgcn_make_buffer_rsrc((void*)((const char*)K + seqoff), (short)0, nrec, 0x00020000);
  const __amdgpu_buffer_rsrc_t srdV = __builtin_amdgcn_make_buffer_rsrc((void*)((const char*)V + seqoff), (short)0, nrec, 0x00020000);
  const __amdgpu_buffer_rsrc_t srdO = __builtin_amdgcn_make_buffer_rsrc((void*)((char*)O + seqoff), (short)0, nrec, 0x00020000);
  const int NT = (seqlen + 31) >> 5;
  const int qrow0 = qb * 128 + (int)wid * 32;
  const bool active = qrow0 < seqlen;

  const unsigned ldsb = (unsigned)(size_t)lds;
  const unsigned drow = 8u * wid + ((lane >> 2) & 7u);
  const unsigned vK = drow * ROWB + 64u * hi + 16u * ((lane & 3u) ^ (2u * (wid & 1u) + ((lane >> 4) & 1u)));
  const unsigned vV = drow * ROWB + 64u * hi + 16u * (lane & 3u);
  auto issue = [&](int t, int slot) {
    const unsigned m = rfl(ldsb + (unsigned)slot * 16384u + wid * 2048u), so = rfl((unsigned)t * 32u * ROWB);
    dma16(m, vK, srdK, so); dma16(m + 1024u, vK, srdK, so + 128u);
    dma16(m + 8192u, vV, srdV, so); dma16(m + 8192u + 1024u, vV, srdV, so + 128u);
  };
  bf16x8 qf[8];
  {
    const unsigned vq = (unsigned)(qrow0 + (int)l32) * ROWB + 16u * hi;
#pragma unroll
    for (int d0 = 0; d0 < 8; ++d0) qf[d0] = __builtin_bit_cast(bf16x8, __builtin_amdgcn_raw_buffer_load_b128(srdQ, vq + 32u * d0, 0, 0));
  }
#pragma unroll
  for (int t = 0; t < NS - 1; ++t) issue(t, t);

  const lds_cptr kb0 = (lds_cptr)lds + 2048u * (l32 >> 3) + 64u * (l32 & 7u) + 16u * ((0u + hi) ^ ((l32 >> 2) & 3u));
  const lds_cptr kb1 = (lds_cptr)lds + 2048u * (l32 >> 3) + 64u * (l32 & 7u) + 16u * ((2u + hi) ^ ((l32 >> 2) & 3u));
  const lds_cptr vb  = (lds_cptr)lds + 8192u + 256u * hi + 64u * ((lane >> 2) & 3u) + 32u * ((lane >> 4) & 1u) + 8u * (lane & 3u);

  f32x16 o[4];
#pragma unroll
  for (int i = 0; i < 4; ++i) o[i] = f32x16{};
  float mhat = -1e30f, lsum = 0.f;
  int slot = 0;
  for (int t = 0; t < NT; ++t) {
    asm volatile("s_waitcnt vmcnt(%0)\n\ts_barrier" :: "i"(4 * (NS - 2)) : "memory");
    if (t == 0) ts1 = __builtin_amdgcn_s_memrealtime();
    { int ns = slot + NS - 1; if (ns >= NS) ns -= NS; issue(t + NS - 1, ns); }
    if (active) {
      const unsigned so = (unsigned)slot * 16384u;
      f32x16 s = f32x16{};
#pragma unroll
      for (int d0 = 0; d0 < 8; ++d0) {
        const bf16x8 kf = *(const __attribute__((address_space(3))) bf16x8*)(((d0 & 1) ? kb1 : kb0) + so + 512 * (d0 >> 1));
        s = MFMA(kf, qf[d0], s);
      }
      if (t == NT - 1) {
        const int rem = seqlen - t * 32 - 4 * (int)hi;
#pragma unroll
        for (int r = 0; r < 16; ++r) if ((r & 3) + 8 * (r >> 2) >= rem) s[r] = -INFINITY;
      }
      float mx = MX3(s[0], s[1], s[2]);
#pragma unroll
      for (int r = 3; r < 15; r += 2) mx = MX3(mx, s[r], s[r + 1]);
      mx = __builtin_fmaxf(mx, s[15]);
      { auto rr = __builtin_amdgcn_permlane32_swap(__float_as_uint(mx), __float_as_uint(mx), false, false);
        mx = __builtin_fmaxf(__uint_as_float(rr[0]), __uint_as_float(rr[1])); }
      const float ms = mx * C2;
      if (__builtin_expect(__any(ms - mhat > THR), 0)) {
        const float mn = __builtin_fmaxf(mhat, ms), f = __builtin_amdgcn_exp2f(mhat - mn);
        mhat = mn; lsum *= f;
#pragma unroll
        for (int i = 0; i < 4; ++i)
#pragma unroll
          for (int r = 0; r < 16; ++r) o[i][r] *= f;
      }
      const float nm = -mhat;
      float ps = 0.f;
#pragma unroll
      for (int r = 0; r < 16; ++r) { s[r] = __builtin_amdgcn_exp2f(__builtin_fmaf(s[r], C2, nm)); ps += s[r]; }
      lsum += ps;
      u32x4 p0 = {cvtpk(s[0], s[1]), cvtpk(s[2], s[3]), cvtpk(s[4], s[5]), cvtpk(s[6], s[7])};
      u32x4 p1 = {cvtpk(s[8], s[9]), cvtpk(s[10], s[11]), cvtpk(s[12], s[13]), cvtpk(s[14], s[15])};
      const bf16x8 pf0 = __builtin_bit_cast(bf16x8, p0), pf1 = __builtin_bit_cast(bf16x8, p1);
#pragma unroll
      for (int db = 0; db < 4; ++db) {
        const s16x4 a0 = vtr(vb + so + 512 * db), a1 = vtr(vb + so + 2048 + 512 * db);
        const s16x4 b0 = vtr(vb + so + 4096 + 512 * db), b1 = vtr(vb + so + 6144 + 512 * db);
        o[db] = MFMA(__builtin_shufflevector(a0, a1, 0, 1, 2, 3, 4, 5, 6, 7), pf0, o[db]);
        o[db] = MFMA(__builtin_shufflevector(b0, b1, 0, 1, 2, 3, 4, 5, 6, 7), pf1, o[db]);
      }
    }
    slot = (slot + 1 == NS) ? 0 : slot + 1;
  }
  if (active) {
    { auto rr = __builtin_amdgcn_permlane32_swap(__float_as_uint(lsum), __float_as_uint(lsum), false, false);
      lsum = __uint_as_float(rr[0]) + __uint_as_float(rr[1]); }
    const float il = 1.0f / lsum;
    const unsigned vo = (unsigned)(qrow0 + (int)l32) * ROWB + 16u * hi;
#pragma unroll
    for (int db = 0; db < 4; ++db)
#pragma unroll
      for (int k = 0; k < 4; k += 2) {
        unsigned a0 = cvtpk(o[db][4 * k + 0] * il, o[db][4 * k + 1] * il), a1 = cvtpk(o[db][4 * k + 2] * il, o[db][4 * k + 3] * il);
        unsigned b0 = cvtpk(o[db][4 * k + 4] * il, o[db][4 * k + 5] * il), b1 = cvtpk(o[db][4 * k + 6] * il, o[db][4 * k + 7] * il);
        auto r0 = __builtin_amdgcn_permlane32_swap(a0, b0, false, false);
        auto r1 = __builtin_amdgcn_permlane32_swap(a1, b1, false, false);
        u32x4 w = {r0[0], r1[0], r0[1], r1[1]};
        __builtin_amdgcn_raw_buffer_store_b128(w, srdO, vo + 64u * db + 16u * k, 0, 0);
      }
  }
  asm volatile("s_waitcnt vmcnt(0)" ::: "memory");
  if (blockIdx.x == 0 && wid == 0 && EXPT != 6) {
    const unsigned long long te = __builtin_amdgcn_s_memrealtime(), ce = __builtin_amdgcn_s_memtime();
    unsigned long long extra = 0;
    if (EXPT == 2) extra = (unsigned)((float)(ce - c0) * 100.f / (float)(te - ts0));
    if (EXPT == 3) extra = te - ts0;
    if (EXPT == 4) extra = 10 * (ts15 - ts0);
    if (EXPT == 5) extra = 5 * (ts1 - ts0);
    while (__builtin_amdgcn_s_memrealtime() < ts0 + 5000 + extra) __builtin_amdgcn_s_sleep(1);
  }
}

extern "C" void kernel_launch(void* const* d_in, const int* in_sizes, int n_in,
                              void* d_out, int out_size, void* d_ws, size_t ws_size,
                              hipStream_t stream) {
  const int T = in_sizes[0] / (H * D), nseq = in_sizes[3] - 1;
  const int maxitems = T / 128 + nseq;
  attn_v0<<<maxitems * H, 256, 0, stream>>>((const unsigned short*)d_in[0], (const unsigned short*)d_in[1], (const unsigned short*)d_in[2],
                                             (const int*)d_in[3], (unsigned short*)d_out, nseq);
}
```

```cpp
#include <hip/hip_runtime.h>

__global__ __launch_bounds__(768, 3) void varlen_attn_kernel(const void* Q, const void* K, const void* V, void* O, const int* cu, void* ws,
                                                             int nseq, unsigned pfstep, unsigned tmax, unsigned zero) {
  unsigned tid = threadIdx.x;
  asm volatile(
    "Ltop_%=:\n\t"
    "s_load_dwordx16 s[8:23], %1, 0x0\n\t"
    "s_getpc_b64 s[4:5]\n\t"
    "v_lshlrev_b32 v100, 7, v0\n\t"
    "v_min_u32 v100, Lcold_%=-Ltop_%=, v100\n\t"
    "v_and_b32 v166, 63, v0\n\t"
    "v_lshrrev_b32 v0, 6, v0\n\t"
    "global_load_dword v100, v100, s[4:5]\n\t"
    "v_readfirstlane_b32 s77, v0\n\t"
    "s_and_b32 s82, %2, 7\n\t"
    "s_lshr_b32 s83, %2, 3\n\t"
    "s_lshl_b32 s82, s82, 9\n\t"
    "s_mul_i32 s81, s83, 3\n\t"
    "v_and_b32 v6, 15, v166\n\t"
    "v_and_b32 v1, 3, v166\n\t"
    "v_lshrrev_b32 v7, 2, v166\n\t"
    "v_lshl_or_b32 v8, v1, 7, s82\n\t"
    "s_lshr_b32 s24, %2, 4\n\t"
    "s_and_b32 s25, s83, 1\n\t"
    "s_lshl_b32 s25, s25, 8\n\t"
    "s_or_b32 s25, s25, s82\n\t"
    "s_and_b32 s76, s77, 7\n\t"
    "s_sub_u32 s84, s77, 1\n\t"
    "s_add_u32 s81, s81, s84\n\t"
    "v_bfe_u32 v152, v166, 2, 3\n\t"
    "v_lshrrev_b32 v1, 5, v166\n\t"
    "v_lshlrev_b32 v152, 12, v152\n\t"
    "v_lshl_or_b32 v152, v1, 6, v152\n\t"
    "v_and_b32 v1, 3, v166\n\t"
    "v_lshl_or_b32 v152, v1, 4, v152\n\t"
    "v_and_b32 v1, 16, v166\n\t"
    "v_xor_b32 v153, v1, v152\n\t"
    "s_mov_b32 s68, 0x3e0293ee\n\t"
    "s_mov_b32 s69, 0x47800000\n\t"
    "v_mov_b32 v165, 163776\n\t"
    "s_cmp_lg_u32 s77, 0\n\t"
    ".p2align 5,,4\n\ts_cbranch_scc1 Lo1_%=\n\t"
    "s_setprio 3\n\t"
    "s_waitcnt lgkmcnt(0)\n\t"
    "s_sub_u32 s80, s20, 1\n\t"
    "v_min_u32 v9, s80, v6\n\t"
    "v_lshlrev_b32 v9, 2, v9\n\t"
    "global_load_dwordx2 v[10:11], v9, s[16:17]\n\t"
    "s_branch Lo2_%=\n\t"
    "Lo1_%=:\n\t"
    "s_waitcnt lgkmcnt(0)\n\t"
    "s_cmp_ge_u32 s84, 3\n\t"
    ".p2align 5,,4\n\ts_cbranch_scc1 Lo2_%=\n\t"
    "s_mul_i32 s81, s81, s21\n\t"
    "v_add_u32 v7, s81, v7\n\t"
    "v_min_u32 v7, s22, v7\n\t"
    "v_lshl_or_b32 v7, v7, 12, v8\n\t"
    "s_nop 7\n\t"
    "global_load_dword v101, v7, s[8:9]\n\t"
    "Lo2_%=:\n\t"
    "s_mov_b32 s36, s8\n\t"
    "s_and_b32 s37, s9, 0xffff\n\t"
    "s_mov_b32 s38, 0\n\t"
    "s_mov_b32 s39, 0x20000\n\t"
    "s_mov_b32 s48, s14\n\t"
    "s_and_b32 s49, s15, 0xffff\n\t"
    "s_mov_b32 s50, 0\n\t"
    "s_mov_b32 s51, 0x20000\n\t"
    "s_sub_u32 s40, s10, 4096\n\t"
    "s_subb_u32 s41, s11, 0\n\t"
    "s_and_b32 s41, s41, 0xffff\n\t"
    "s_mov_b32 s42, 0\n\t"
    "s_mov_b32 s43, 0x20000\n\t"
    "s_sub_u32 s44, s12, 4096\n\t"
    "s_subb_u32 s45, s13, 0\n\t"
    "s_and_b32 s45, s45, 0xffff\n\t"
    "s_mov_b32 s46, 0\n\t"
    "s_mov_b32 s47, 0x20000\n\t"
    "s_cmp_lg_u32 s77, 0\n\t"
    ".p2align 5,,4\n\ts_cbranch_scc1 Lbox_%=\n\t"
    "v_cmp_ne_u32_e64 s[6:7], 0, v1\n\t"
    "v_cmp_gt_u32_e64 s[4:5], s20, v6\n\t"
    "v_lshlrev_b32 v12, 1, v6\n\t"
    "v_sub_u32 v13, 30, v12\n\t"
    "v_sub_u32 v12, 31, v12\n\t"
    "s_mov_b32 s85, 0x80008000\n\t"
    "v_mov_b32 v2, -1\n\t"
    "v_mov_b32 v4, 163784\n\t"
    "ds_write_b32 v165, v2\n\t"
    "s_sub_u32 s82, 32, s24\n\t"
    "s_add_u32 s83, s24, 1\n\t"
    "s_cmp_gt_i32 s20, 16\n\t"
    ".p2align 5,,4\n\ts_cbranch_scc1 Lboxw_%=\n\t"
    "s_waitcnt vmcnt(0)\n\t"
    "v_sub_u32 v14, v11, v10\n\t"
    "v_cndmask_b32_e64 v14, 0, v14, s[4:5]\n\t"
    "v_add_u32 v15, 31, v14\n\t"
    "v_lshrrev_b32 v15, 5, v15\n\t"
    "v_add_u32 v28, -5, v15\n\t"
    "v_cmp_gt_u32_e64 s[4:5], 4, v28\n\t"
    "v_lshl_or_b32 v16, v14, 4, v6\n\t"
    "v_cmp_lt_u32_e32 vcc, 16, v15\n\t"
    "v_cndmask_b32_e64 v16, -1, v16, s[4:5]\n\t"
    ".p2align 5,,4\n\ts_cbranch_vccnz Lboxw_%=\n\t"
    "v_cmp_lt_u32_e32 vcc, 8, v15\n\t"
    "v_min_u32_dpp v17, v16, v16 row_ror:8 row_mask:0xf bank_mask:0xf\n\t"
    "v_max_u32_dpp v32, v15, v15 row_ror:8 row_mask:0xf bank_mask:0xf\n\t"
    "v_add_u32 v28, -4, v15\n\t"
    "v_min_u32_dpp v17, v17, v17 row_ror:4 row_mask:0xf bank_mask:0xf\n\t"
    "v_max_u32_dpp v32, v32, v32 row_ror:4 row_mask:0xf bank_mask:0xf\n\t"
    "v_max_i32 v28, 7, v28\n\t"
    "v_min_u32_dpp v17, v17, v17 row_ror:2 row_mask:0xf bank_mask:0xf\n\t"
    "v_max_u32_dpp v32, v32, v32 row_ror:2 row_mask:0xf bank_mask:0xf\n\t"
    "v_cndmask_b32_e32 v19, v15, v28, vcc\n\t"
    "v_min_u32_dpp v17, v17, v17 row_ror:1 row_mask:0xf bank_mask:0xf\n\t"
    "v_max_u32_dpp v32, v32, v32 row_ror:1 row_mask:0xf bank_mask:0xf\n\t"
    "v_cmp_lt_u32_e64 s[30:31], 12, v15\n\t"
    "v_cmp_eq_u32_e32 vcc, v16, v17\n\t"
    "v_add_u32 v18, 1, v15\n\t"
    "v_lshrrev_b32 v18, 1, v18\n\t"
    "s_and_b64 vcc, vcc, s[4:5]\n\t"
    "s_or_b64 vcc, vcc, s[30:31]\n\t"
    "v_cndmask_b32_e32 v19, v19, v18, vcc\n\t"
    "v_sub_u32 v20, v15, v19\n\t"
    "v_cmp_lt_u32_e64 s[4:5], 4, v19\n\t"
    "v_cmp_ne_u32_e64 s[30:31], 0, v20\n\t"
    "v_cmp_lt_u32_e32 vcc, 4, v20\n\t"
    "v_mad_u32_u24 v21, v15, 10, 1\n\t"
    "v_mul_u32_u24 v22, 6, v15\n\t"
    "v_cndmask_b32_e64 v23, v22, v21, s[4:5]\n\t"
    "v_cndmask_b32_e64 v24, 0, v22, s[30:31]\n\t"
    "v_cndmask_b32_e32 v24, v24, v21, vcc\n\t"
    "v_lshl_or_b32 v23, v23, 5, v12\n\t"
    "v_lshl_or_b32 v24, v24, 5, v13\n\t"
    "v_lshl_or_b32 v25, v24, 16, v23\n\t"
    "v_sub_u32 v25, 0, v25\n\t"
    "v_cndmask_b32_e64 v26, v23, v24, s[6:7]\n\t"
    "v_lshl_or_b32 v26, v26, 16, v26\n\t"
    "v_mov_b32_dpp v25, v25 row_ror:8 row_mask:0xc bank_mask:0xf\n\t"
    "v_or_b32 v26, s85, v26\n\t"
    "v_add_u32 v28, v25, v26\n\t"
    "v_add_u32_dpp v29, v25, v26 row_ror:1 row_mask:0xf bank_mask:0xf\n\t"
    "v_add_u32_dpp v30, v25, v26 row_ror:2 row_mask:0xf bank_mask:0xf\n\t"
    "v_add_u32_dpp v31, v25, v26 row_ror:3 row_mask:0xf bank_mask:0xf\n\t"
    "v_add_u32_dpp v23, v25, v26 row_ror:4 row_mask:0xf bank_mask:0xf\n\t"
    "v_add_u32_dpp v24, v25, v26 row_ror:5 row_mask:0xf bank_mask:0xf\n\t"
    "v_add_u32_dpp v21, v25, v26 row_ror:6 row_mask:0xf bank_mask:0xf\n\t"
    "v_add_u32_dpp v22, v25, v26 row_ror:7 row_mask:0xf bank_mask:0xf\n\t"
    "v_and_b32 v28, s85, v28\n\t"
    "v_and_b32 v29, s85, v29\n\t"
    "v_and_b32 v30, s85, v30\n\t"
    "v_and_b32 v31, s85, v31\n\t"
    "v_and_b32 v23, s85, v23\n\t"
    "v_and_b32 v24, s85, v24\n\t"
    "v_and_b32 v21, s85, v21\n\t"
    "v_and_b32 v22, s85, v22\n\t"
    "v_bcnt_u32_b32 v27, v28, 0\n\t"
    "v_bcnt_u32_b32 v17, v29, 0\n\t"
    "v_bcnt_u32_b32 v27, v30, v27\n\t"
    "v_bcnt_u32_b32 v17, v31, v17\n\t"
    "v_bcnt_u32_b32 v27, v23, v27\n\t"
    "v_bcnt_u32_b32 v17, v24, v17\n\t"
    "v_bcnt_u32_b32 v27, v21, v27\n\t"
    "v_bcnt_u32_b32 v17, v22, v17\n\t"
    "v_add_u32 v27, v27, v17\n\t"
    "v_mov_b32 v17, v27\n\t"
    "v_cndmask_b32_e64 v28, v19, v20, s[6:7]\n\t"
    "v_cndmask_b32_e64 v29, 0, v19, s[6:7]\n\t"
    "v_permlane32_swap_b32 v27, v17\n\t"
    "v_lshl_or_b32 v28, v29, 4, v28\n\t"
    "v_add_u32 v27, v27, v17\n\t"
    "v_cmp_eq_u32_e32 vcc, s82, v27\n\t"
    "v_cmp_eq_u32_e64 s[4:5], s83, v27\n\t"
    "v_lshl_or_b32 v28, v14, 8, v28\n\t"
    "v_lshl_or_b32 v11, v32, 20, v28\n\t"
    "v_cndmask_b32_e32 v4, v4, v165, vcc\n\t"
    "s_or_b64 exec, vcc, s[4:5]\n\t"
    "ds_write_b64 v4, v[10:11]\n\t"
    "s_mov_b64 exec, -1\n\t"
    "Lboxw_%=:\n\t"
    "s_waitcnt lgkmcnt(0)\n\t"
    "s_setprio 0\n\t"
    "Lbox_%=:\n\t"
    "s_barrier\n\t"
    "ds_read_b128 v[12:15], v165\n\t"
    "s_mov_b32 s26, 0\n\t"
    "s_waitcnt lgkmcnt(0)\n\t"
    "v_readfirstlane_b32 s5, v12\n\t"
    "v_readfirstlane_b32 s4, v13\n\t"
    "v_readfirstlane_b32 s31, v14\n\t"
    "v_readfirstlane_b32 s30, v15\n\t"
    "s_cmp_lt_i32 s5, 0\n\t"
    ".p2align 5,,4\n\ts_cbranch_scc1 Lgeneric_%=\n\t"
    "s_and_b32 s80, s30, 15\n\t"
    "s_cselect_b32 s80, s30, 0\n\t"
    "s_cmp_gt_u32 s80, s4\n\t"
    "s_cselect_b64 s[28:29], s[30:31], s[4:5]\n\t"
    "s_cselect_b64 s[30:31], s[4:5], s[30:31]\n\t"
    "s_and_b32 s55, s28, 15\n\t"
    ".p2align 5,,4\n\ts_cbranch_scc0 Ldone_%=\n\t"
    "s_bfe_u32 s52, s28, 0xc0008\n\t"
    "s_lshl_b32 s53, s29, 12\n\t"
    "s_or_b32 s53, s53, s25\n\t"
    "s_bfe_u32 s54, s28, 0x40004\n\t"
    "s_and_b32 s59, s30, 15\n\t"
    "s_bfe_u32 s56, s30, 0xc0008\n\t"
    "s_lshl_b32 s57, s31, 12\n\t"
    "s_or_b32 s57, s57, s25\n\t"
    "s_xor_b32 s57, s57, 256\n\t"
    "s_bfe_u32 s58, s30, 0x40004\n\t"
    "s_lshr_b32 s94, s28, 20\n\t"
    "s_add_u32 s80, s55, s59\n\t"
    "s_min_u32 s81, s55, s59\n\t"
    "s_cmp_gt_u32 s81, 4\n\t"
    "s_cselect_b32 s80, 99, s80\n\t"
    "s_cmp_le_u32 s52, 64\n\t"
    "s_cselect_b32 s80, 99, s80\n\t"
    "s_cmp_gt_u32 s80, 11\n\t"
    ".p2align 5,,4\n\ts_cbranch_scc1 Lm3_%=\n\t"
    "Lround_%=:\n\t"
    "s_cmp_gt_u32 s59, s55\n\t"
    "s_cselect_b32 s99, 8, 0\n\t"
    "s_xor_b32 s99, s99, s77\n\t"
    "s_and_b32 s99, s99, 8\n\t"
    "s_cselect_b64 s[64:65], s[56:57], s[52:53]\n\t"
    "s_cselect_b64 s[66:67], s[58:59], s[54:55]\n\t"
    "s_cmp_ge_u32 s76, s67\n\t"
    ".p2align 5,,4\n\ts_cbranch_scc1 Lhelper_%=\n\t"
    "s_lshl_b32 s84, s64, 12\n\t"
    "s_add_u32 s84, s84, s65\n\t"
    "s_add_u32 s38, s84, 4294963456\n\t"
    "s_add_u32 s79, s66, s76\n\t"
    "s_lshl_b32 s79, s79, 17\n\t"
    "s_add_u32 s82, s79, s65\n\t"
    "s_lshl_b32 s83, s76, 13\n\t"
    "s_cmp_lg_u32 s99, 0\n\t"
    ".p2align 5,,4\n\ts_cbranch_scc1 Lp4_%=\n\t"
    "s_mov_b32 m0, s83\n\t"
    "s_add_u32 s83, s83, 896\n\t"
    "buffer_load_dwordx4 v153, s[36:39], s82 offen nt lds\n\t"
    "s_mov_b32 m0, s83\n\t"
    "s_add_u32 s83, s83, 1152\n\t"
    "buffer_load_dwordx4 v153, s[36:39], s82 offen offset:128 nt lds\n\t"
    "v_xor_b32 v153, 32, v153\n\t"
    "s_add_u32 s82, s82, 0x8000\n\t"
    "s_mov_b32 m0, s83\n\t"
    "s_add_u32 s83, s83, 896\n\t"
    "buffer_load_dwordx4 v153, s[36:39], s82 offen nt lds\n\t"
    "s_mov_b32 m0, s83\n\t"
    "s_add_u32 s83, s83, 1152\n\t"
    "buffer_load_dwordx4 v153, s[36:39], s82 offen offset:128 nt lds\n\t"
    "v_xor_b32 v153, 32, v153\n\t"
    "s_add_u32 s82, s82, 0x8000\n\t"
    "s_mov_b32 m0, s83\n\t"
    "s_add_u32 s83, s83, 896\n\t"
    "buffer_load_dwordx4 v153, s[36:39], s82 offen nt lds\n\t"
    "s_mov_b32 m0, s83\n\t"
    "s_add_u32 s83, s83, 1152\n\t"
    "buffer_load_dwordx4 v153, s[36:39], s82 offen offset:128 nt lds\n\t"
    "v_xor_b32 v153, 32, v153\n\t"
    "s_add_u32 s82, s82, 0x8000\n\t"
    "s_mov_b32 m0, s83\n\t"
    "s_add_u32 s83, s83, 896\n\t"
    "buffer_load_dwordx4 v153, s[36:39], s82 offen nt lds\n\t"
    "s_mov_b32 m0, s83\n\t"
    "s_add_u32 s83, s83, 1152\n\t"
    "buffer_load_dwordx4 v153, s[36:39], s82 offen offset:128 nt lds\n\t"
    "v_xor_b32 v153, 32, v153\n\t"
    "s_add_u32 s82, s82, 0x8000\n\t"
    "Lp4_%=:\n\t"
    "s_add_u32 s95, s52, 31\n\t"
    "s_lshr_b32 s95, s95, 5\n\t"
    "s_add_u32 s96, s56, 31\n\t"
    "s_lshr_b32 s96, s96, 5\n\t"
    "s_cmp_eq_u32 s59, 0\n\t"
    "s_cselect_b32 s96, 0, s96\n\t"
    "s_cselect_b32 s3, s55, s59\n\t"
    "s_cmp_gt_u32 s3, 4\n\t"
    "s_cselect_b32 s3, 1, 0\n\t"
    "s_cselect_b32 s21, 0, s96\n\t"
    "s_cmp_ge_u32 s21, 3\n\t"
    "s_cselect_b32 s3, 1, s3\n\t"
    "s_min_u32 s21, s21, 1\n\t"
    "s_add_u32 s79, s79, s65\n\t"
    "s_lshl_b32 s78, s76, 13\n\t"
    "s_mov_b32 s50, s38\n\t"
    "s_sub_u32 s74, s64, 1\n\t"
    "s_and_b32 s80, s74, 31\n\t"
    "s_lshr_b32 s74, s74, 5\n\t"
    "s_mov_b32 s71, 0\n\t"
    "s_mov_b32 s85, 4294885376\n\t"
    "v_lshlrev_b32 v112, 26, v166\n\t"
    "v_ashrrev_i32 v112, 26, v112\n\t"
    "v_cmp_lt_i32_e32 vcc, s80, v112\n\t"
    "v_mov_b32 v162, 0xff800000\n\t"
    "v_mov_b32 v161, 1.0\n\t"
    "v_cndmask_b32_e32 v162, 0, v162, vcc\n\t"
    "v_mov_b64 v[144:145], 0\n\t"
    "v_mov_b64 v[146:147], 0\n\t"
    "v_lshrrev_b32 v115, 5, v166\n\t"
    "v_bfe_u32 v112, v166, 3, 2\n\t"
    "v_lshlrev_b32 v163, 11, v112\n\t"
    "v_and_b32 v112, 7, v166\n\t"
    "v_lshl_or_b32 v163, v112, 6, v163\n\t"
    "v_bfe_u32 v113, v166, 2, 2\n\t"
    "v_xor_b32 v114, v113, v115\n\t"
    "v_lshl_or_b32 v163, v114, 4, v163\n\t"
    "v_lshlrev_b32 v165, 6, v113\n\t"
    "v_lshl_or_b32 v165, v115, 8, v165\n\t"
    "v_and_b32 v113, 16, v166\n\t"
    "v_lshl_or_b32 v165, v113, 1, v165\n\t"
    "v_and_b32 v113, 3, v166\n\t"
    "v_lshl_or_b32 v165, v113, 3, v165\n\t"
    "v_or_b32 v165, 73728, v165\n\t"
    "v_add_u32 v154, s78, v163\n\t"
    "v_xor_b32 v155, 32, v154\n\t"
    "v_or_b32 v163, 65536, v163\n\t"
    "v_mov_b32 v160, 0\n\t"
    "s_cmp_lg_u32 s99, 0\n\t"
    ".p2align 5,,4\n\ts_cbranch_scc1 Lsecond_%=\n\t"
    "v_xor_b32_e32 v164, 32, v163\n\t"
    "s_sub_u32 s75, s96, s3\n\t"
    "s_add_u32 s75, s75, s21\n\t"
    "s_mov_b32 s21, 0\n\t"
    "s_cmp_eq_u32 s96, 0\n\t"
    "s_cselect_b32 s75, 0, s75\n\t"
    "s_cselect_b32 s3, s3, 0\n\t"
    "s_waitcnt vmcnt(8)\n\t"
    "v_mfma_f32_32x32x2_f32 v[96:111], v162, v161, 0\n\t"
    "v_mfma_f32_32x32x16_bf16 v[0:15], v[144:147], v[144:147], 0\n\t"
    "v_mfma_f32_32x32x16_bf16 v[16:31], v[144:147], v[144:147], 0\n\t"
    "v_mfma_f32_32x32x16_bf16 v[32:47], v[144:147], v[144:147], 0\n\t"
    "v_mfma_f32_32x32x16_bf16 v[48:63], v[144:147], v[144:147], 0\n\t"
    "s_barrier\n\t"
    "ds_read_b128 v[128:131], v163 offset:0\n\t"
    "ds_read_b128 v[132:135], v164 offset:0\n\t"
    "ds_read_b128 v[136:139], v163 offset:512\n\t"
    "ds_read_b128 v[140:143], v164 offset:512\n\t"
    "ds_read_b128 v[112:115], v163 offset:1024\n\t"
    "ds_read_b128 v[116:119], v164 offset:1024\n\t"
    "ds_read_b128 v[120:123], v163 offset:1536\n\t"
    "ds_read_b128 v[124:127], v164 offset:1536\n\t"
    "s_waitcnt vmcnt(0)\n\t"
    "ds_read_b128 v[64:67], v154 offset:0\n\t"
    "ds_read_b128 v[68:71], v155 offset:0\n\t"
    "ds_read_b128 v[72:75], v154 offset:512\n\t"
    "ds_read_b128 v[76:79], v155 offset:512\n\t"
    "ds_read_b128 v[80:83], v154 offset:1024\n\t"
    "ds_read_b128 v[84:87], v155 offset:1024\n\t"
    "ds_read_b128 v[88:91], v154 offset:1536\n\t"
    "ds_read_b128 v[92:95], v155 offset:1536\n\t"
    "Lfirst_%=:\n\t"
    "s_waitcnt lgkmcnt(7)\n\t"
    "v_mfma_f32_32x32x16_bf16 v[96:111], v[128:131], v[64:67], v[96:111]\n\t"
    "s_waitcnt lgkmcnt(6)\n\t"
    "v_mfma_f32_32x32x16_bf16 v[96:111], v[132:135], v[68:71], v[96:111]\n\t"
    "s_waitcnt lgkmcnt(5)\n\t"
    "v_mfma_f32_32x32x16_bf16 v[96:111], v[136:139], v[72:75], v[96:111]\n\t"
    "s_waitcnt lgkmcnt(4)\n\t"
    "v_mfma_f32_32x32x16_bf16 v[96:111], v[140:143], v[76:79], v[96:111]\n\t"
    "s_waitcnt lgkmcnt(3)\n\t"
    "v_mfma_f32_32x32x16_bf16 v[96:111], v[112:115], v[80:83], v[96:111]\n\t"
    "s_waitcnt lgkmcnt(2)\n\t"
    "v_mfma_f32_32x32x16_bf16 v[96:111], v[116:119], v[84:87], v[96:111]\n\t"
    "s_waitcnt lgkmcnt(1)\n\t"
    "v_mfma_f32_32x32x16_bf16 v[96:111], v[120:123], v[88:91], v[96:111]\n\t"
    "s_waitcnt lgkmcnt(0)\n\t"
    "v_mfma_f32_32x32x16_bf16 v[96:111], v[124:127], v[92:95], v[96:111]\n\t"
    "s_add_u32 s71, s71, 0x4000\n\t"
    "s_cmp_eq_u32 s71, 98304\n\t"
    "s_cselect_b32 s72, s85, 0x4000\n\t"
    "s_cselect_b32 s71, 0, s71\n\t"
    "v_add_u32_e32 v163, s72, v163\n\t"
    "v_xor_b32_e32 v164, 32, v163\n\t"
    "s_add_u32 s80, s66, s76\n\t"
    "s_lshl_b32 s80, s80, 5\n\t"
    "s_sub_u32 s80, s64, s80\n\t"
    "v_and_b32 v154, 31, v166\n\t"
    "v_cmp_gt_u32_e32 vcc, s80, v154\n\t"
    "v_mov_b32 v154, 0xff800000\n\t"
    "v_max3_f32 v152, v96, v97, v98\n\t"
    "v_max3_f32 v153, v99, v100, v101\n\t"
    "v_max3_f32 v152, v152, v102, v103\n\t"
    "v_max3_f32 v153, v153, v104, v105\n\t"
    "v_max3_f32 v152, v152, v106, v107\n\t"
    "v_max3_f32 v153, v153, v108, v109\n\t"
    "v_max3_f32 v152, v152, v110, v111\n\t"
    "v_max_f32_e32 v152, v152, v153\n\t"
    "v_mov_b32 v153, v152\n\t"
    "s_nop 1\n\t"
    "v_permlane32_swap_b32 v152, v153\n\t"
    "s_nop 0\n\t"
    "v_max_f32_e32 v152, v152, v153\n\t"
    "v_mul_f32_e64 v162, -v152, s68\n\t"
    "v_cndmask_b32_e32 v162, v154, v162, vcc\n\t"
    "v_fma_f32 v112, v96, s68, v162\n\t"
    "v_fma_f32 v113, v97, s68, v162\n\t"
    "v_fma_f32 v114, v98, s68, v162\n\t"
    "v_fma_f32 v115, v99, s68, v162\n\t"
    "v_fma_f32 v116, v100, s68, v162\n\t"
    "v_fma_f32 v117, v101, s68, v162\n\t"
    "v_fma_f32 v118, v102, s68, v162\n\t"
    "v_fma_f32 v119, v103, s68, v162\n\t"
    "v_fma_f32 v120, v104, s68, v162\n\t"
    "v_fma_f32 v121, v105, s68, v162\n\t"
    "v_fma_f32 v122, v106, s68, v162\n\t"
    "v_fma_f32 v123, v107, s68, v162\n\t"
    "v_fma_f32 v124, v108, s68, v162\n\t"
    "v_fma_f32 v125, v109, s68, v162\n\t"
    "v_fma_f32 v126, v110, s68, v162\n\t"
    "v_fma_f32 v127, v111, s68, v162\n\t"
    "s_setprio 1\n\t"
    "s_barrier\n\t"
    "ds_read_b128 v[128:131], v163 offset:0\n\t"
    "ds_read_b128 v[132:135], v164 offset:0\n\t"
    "ds_read_b128 v[136:139], v163 offset:512\n\t"
    "ds_read_b128 v[140:143], v164 offset:512\n\t"
    "v_exp_f32_e32 v152, v112\n\t"
    "v_exp_f32_e32 v153, v113\n\t"
    "v_exp_f32_e32 v154, v114\n\t"
    "v_exp_f32_e32 v155, v115\n\t"
    "v_add_f32_e32 v161, v152, v153\n\t"
    "v_exp_f32_e32 v156, v116\n\t"
    "v_exp_f32_e32 v157, v117\n\t"
    "v_add_f32_e32 v161, v154, v161\n\t"
    "v_cvt_pk_bf16_f32 v144, v152, v153\n\t"
    "s_mov_b32 s89, 0\n\t"
    "s_sub_u32 s74, s74, s3\n\t"
    "s_sub_u32 s74, s74, 1\n\t"
    ".p2align 5,,4\n\ts_cbranch_scc1 Ld5_%=\n\t"
    "s_waitcnt lgkmcnt(3)\n\t"
    "s_branch Lenter_%=\n\t"
    ".p2align 6\n\t"
    "Lloop_%=:\n\t"
    "ds_read_b128 v[132:135], v164 offset:0\n\t"
    "v_exp_f32_e32 v152, v112\n\t"
    "v_exp_f32_e32 v153, v113\n\t"
    "v_add_f32_e32 v160, v160, v161\n\t"
    "v_mfma_f32_32x32x16_bf16 v[32:47], v[136:139], v[148:151], v[32:47]\n\t"
    "s_waitcnt lgkmcnt(1)\n\t"
    "ds_read_b128 v[136:139], v163 offset:512\n\t"
    "v_exp_f32_e32 v154, v114\n\t"
    "v_exp_f32_e32 v155, v115\n\t"
    "v_add_f32_e32 v161, v152, v153\n\t"
    "v_add_u32_e32 v165, s73, v165\n\t"
    "v_mfma_f32_32x32x16_bf16 v[48:63], v[140:143], v[148:151], v[48:63]\n\t"
    "ds_read_b128 v[140:143], v164 offset:512\n\t"
    "v_exp_f32_e32 v156, v116\n\t"
    "v_exp_f32_e32 v157, v117\n\t"
    "v_add_f32_e32 v161, v154, v161\n\t"
    "v_cvt_pk_bf16_f32 v144, v152, v153\n\t"
    "Lenter_%=:\n\t"
    "v_mfma_f32_32x32x16_bf16 v[96:111], v[128:131], v[64:67], 0\n\t"
    "s_waitcnt lgkmcnt(1)\n\t"
    "ds_read_b128 v[128:131], v163 offset:1024\n\t"
    "v_exp_f32_e32 v158, v118\n\t"
    "v_exp_f32_e32 v159, v119\n\t"
    "v_add_f32_e32 v161, v155, v161\n\t"
    "v_cvt_pk_bf16_f32 v145, v154, v155\n\t"
    "v_mfma_f32_32x32x16_bf16 v[96:111], v[132:135], v[68:71], v[96:111]\n\t"
    "s_mov_b32 s73, s72\n\t"
    "s_add_u32 s71, s71, 0x4000\n\t"
    "ds_read_b128 v[132:135], v164 offset:1024\n\t"
    "v_exp_f32_e32 v152, v120\n\t"
    "v_add_f32_e32 v161, v156, v161\n\t"
    "v_add_f32_e32 v161, v157, v161\n\t"
    "v_cvt_pk_bf16_f32 v146, v156, v157\n\t"
    "v_mfma_f32_32x32x16_bf16 v[96:111], v[136:139], v[72:75], v[96:111]\n\t"
    "s_waitcnt lgkmcnt(1)\n\t"
    "ds_read_b128 v[136:139], v163 offset:1536\n\t"
    "v_exp_f32_e32 v153, v121\n\t"
    "v_exp_f32_e32 v154, v122\n\t"
    "v_add_f32_e32 v161, v158, v161\n\t"
    "v_add_f32_e32 v161, v159, v161\n\t"
    "v_mfma_f32_32x32x16_bf16 v[96:111], v[140:143], v[76:79], v[96:111]\n\t"
    "s_cmp_eq_u32 s71, 98304\n\t"
    "s_cselect_b32 s72, s85, 0x4000\n\t"
    "ds_read_b128 v[140:143], v164 offset:1536\n\t"
    "v_exp_f32_e32 v155, v123\n\t"
    "v_exp_f32_e32 v156, v124\n\t"
    "v_cvt_pk_bf16_f32 v147, v158, v159\n\t"
    "v_add_f32_e32 v161, v152, v161\n\t"
    "v_mfma_f32_32x32x16_bf16 v[96:111], v[128:131], v[80:83], v[96:111]\n\t"
    "s_waitcnt lgkmcnt(1)\n\t"
    "ds_read_b64_tr_b16 v[128:129], v165 offset:0\n\t"
    "ds_read_b64_tr_b16 v[130:131], v165 offset:2048\n\t"
    "v_add_f32_e32 v161, v153, v161\n\t"
    "v_exp_f32_e32 v157, v125\n\t"
    "v_add_f32_e32 v161, v154, v161\n\t"
    "v_mfma_f32_32x32x16_bf16 v[96:111], v[132:135], v[84:87], v[96:111]\n\t"
    "s_cselect_b32 s71, 0, s71\n\t"
    "ds_read_b64_tr_b16 v[132:133], v165 offset:512\n\t"
    "ds_read_b64_tr_b16 v[134:135], v165 offset:2560\n\t"
    "v_exp_f32_e32 v158, v126\n\t"
    "v_exp_f32_e32 v159, v127\n\t"
    "v_mfma_f32_32x32x16_bf16 v[96:111], v[136:139], v[88:91], v[96:111]\n\t"
    "s_waitcnt lgkmcnt(2)\n\t"
    "ds_read_b64_tr_b16 v[136:137], v165 offset:1024\n\t"
    "ds_read_b64_tr_b16 v[138:139], v165 offset:3072\n\t"
    "v_add_f32_e32 v161, v155, v161\n\t"
    "v_add_f32_e32 v161, v156, v161\n\t"
    "v_add_f32_e32 v161, v157, v161\n\t"
    "v_cvt_pk_bf16_f32 v148, v152, v153\n\t"
    "v_mfma_f32_32x32x16_bf16 v[96:111], v[140:143], v[92:95], v[96:111]\n\t"
    "ds_read_b64_tr_b16 v[140:141], v165 offset:1536\n\t"
    "ds_read_b64_tr_b16 v[142:143], v165 offset:3584\n\t"
    "v_add_f32_e32 v161, v158, v161\n\t"
    "v_add_f32_e32 v161, v159, v161\n\t"
    "v_cmp_lt_f32_e32 vcc, s69, v161\n\t"
    "v_add_u32_e32 v163, s72, v163\n\t"
    ".p2align 5,,4\n\ts_cbranch_vccnz Lfb_%=\n\t"
    "Lfbr0_%=:\n\t"
    "v_mfma_f32_32x32x16_bf16 v[0:15], v[128:131], v[144:147], v[0:15]\n\t"
    "s_waitcnt lgkmcnt(2)\n\t"
    "ds_read_b64_tr_b16 v[128:129], v165 offset:4096\n\t"
    "ds_read_b64_tr_b16 v[130:131], v165 offset:6144\n\t"
    "v_cvt_pk_bf16_f32 v149, v154, v155\n\t"
    "v_cvt_pk_bf16_f32 v150, v156, v157\n\t"
    "v_cvt_pk_bf16_f32 v151, v158, v159\n\t"
    "v_xor_b32_e32 v164, 32, v163\n\t"
    "v_mfma_f32_32x32x16_bf16 v[16:31], v[132:135], v[144:147], v[16:31]\n\t"
    "s_sub_u32 s74, s74, 1\n\t"
    "ds_read_b64_tr_b16 v[132:133], v165 offset:4608\n\t"
    "ds_read_b64_tr_b16 v[134:135], v165 offset:6656\n\t"
    "v_fma_f32 v112, v96, s68, v162\n\t"
    "v_fma_f32 v113, v97, s68, v162\n\t"
    "v_fma_f32 v114, v98, s68, v162\n\t"
    "v_fma_f32 v115, v99, s68, v162\n\t"
    "v_mfma_f32_32x32x16_bf16 v[32:47], v[136:139], v[144:147], v[32:47]\n\t"
    "s_waitcnt lgkmcnt(2)\n\t"
    "ds_read_b64_tr_b16 v[136:137], v165 offset:5120\n\t"
    "ds_read_b64_tr_b16 v[138:139], v165 offset:7168\n\t"
    "v_fma_f32 v116, v100, s68, v162\n\t"
    "v_fma_f32 v117, v101, s68, v162\n\t"
    "v_fma_f32 v118, v102, s68, v162\n\t"
    "v_fma_f32 v119, v103, s68, v162\n\t"
    "v_mfma_f32_32x32x16_bf16 v[48:63], v[140:143], v[144:147], v[48:63]\n\t"
    "ds_read_b64_tr_b16 v[140:141], v165 offset:5632\n\t"
    "ds_read_b64_tr_b16 v[142:143], v165 offset:7680\n\t"
    "v_fma_f32 v120, v104, s68, v162\n\t"
    "v_fma_f32 v121, v105, s68, v162\n\t"
    "v_fma_f32 v122, v106, s68, v162\n\t"
    "v_fma_f32 v123, v107, s68, v162\n\t"
    "v_mfma_f32_32x32x16_bf16 v[0:15], v[128:131], v[148:151], v[0:15]\n\t"
    "s_waitcnt lgkmcnt(2)\n\t"
    "s_barrier\n\t"
    "ds_read_b128 v[128:131], v163 offset:0\n\t"
    "v_fma_f32 v124, v108, s68, v162\n\t"
    "v_fma_f32 v125, v109, s68, v162\n\t"
    "v_fma_f32 v126, v110, s68, v162\n\t"
    "v_fma_f32 v127, v111, s68, v162\n\t"
    "v_mfma_f32_32x32x16_bf16 v[16:31], v[132:135], v[148:151], v[16:31]\n\t"
    ".p2align 5,,4\n\ts_cbranch_scc0 Lloop_%=\n\t"
    "s_cmp_eq_u32 s3, 0\n\t"
    ".p2align 5,,4\n\ts_cbranch_scc1 Lexit_%=\n\t"
    "s_mov_b32 s89, 2\n\t"
    "ds_read_b128 v[132:135], v164 offset:0\n\t"
    "v_exp_f32_e32 v152, v112\n\t"
    "v_exp_f32_e32 v153, v113\n\t"
    "v_add_f32_e32 v160, v160, v161\n\t"
    "v_mfma_f32_32x32x16_bf16 v[32:47], v[136:139], v[148:151], v[32:47]\n\t"
    "s_waitcnt lgkmcnt(1)\n\t"
    "ds_read_b128 v[136:139], v163 offset:512\n\t"
    "v_exp_f32_e32 v154, v114\n\t"
    "v_exp_f32_e32 v155, v115\n\t"
    "v_add_f32_e32 v161, v152, v153\n\t"
    "v_add_u32_e32 v165, s73, v165\n\t"
    "v_mfma_f32_32x32x16_bf16 v[48:63], v[140:143], v[148:151], v[48:63]\n\t"
    "ds_read_b128 v[140:143], v164 offset:512\n\t"
    "v_exp_f32_e32 v156, v116\n\t"
    "v_exp_f32_e32 v157, v117\n\t"
    "v_add_f32_e32 v161, v154, v161\n\t"
    "v_cvt_pk_bf16_f32 v144, v152, v153\n\t"
    "v_mfma_f32_32x32x16_bf16 v[96:111], v[128:131], v[64:67], 0\n\t"
    "s_waitcnt lgkmcnt(1)\n\t"
    "ds_read_b128 v[128:131], v163 offset:1024\n\t"
    "v_exp_f32_e32 v158, v118\n\t"
    "v_exp_f32_e32 v159, v119\n\t"
    "v_add_f32_e32 v161, v155, v161\n\t"
    "v_cvt_pk_bf16_f32 v145, v154, v155\n\t"
    "v_mfma_f32_32x32x16_bf16 v[96:111], v[132:135], v[68:71], v[96:111]\n\t"
    "s_mov_b32 s73, s72\n\t"
    "s_add_u32 s71, s71, 0x4000\n\t"
    "ds_read_b128 v[132:135], v164 offset:1024\n\t"
    "v_exp_f32_e32 v152, v120\n\t"
    "v_add_f32_e32 v161, v156, v161\n\t"
    "v_add_f32_e32 v161, v157, v161\n\t"
    "v_cvt_pk_bf16_f32 v146, v156, v157\n\t"
    "v_mfma_f32_32x32x16_bf16 v[96:111], v[136:139], v[72:75], v[96:111]\n\t"
    "s_waitcnt lgkmcnt(1)\n\t"
    "ds_read_b128 v[136:139], v163 offset:1536\n\t"
    "v_exp_f32_e32 v153, v121\n\t"
    "v_exp_f32_e32 v154, v122\n\t"
    "v_add_f32_e32 v161, v158, v161\n\t"
    "v_add_f32_e32 v161, v159, v161\n\t"
    "v_mfma_f32_32x32x16_bf16 v[96:111], v[140:143], v[76:79], v[96:111]\n\t"
    "s_cmp_eq_u32 s71, 98304\n\t"
    "s_cselect_b32 s72, s85, 0x4000\n\t"
    "ds_read_b128 v[140:143], v164 offset:1536\n\t"
    "v_exp_f32_e32 v155, v123\n\t"
    "v_exp_f32_e32 v156, v124\n\t"
    "v_cvt_pk_bf16_f32 v147, v158, v159\n\t"
    "v_add_f32_e32 v161, v152, v161\n\t"
    "v_mfma_f32_32x32x16_bf16 v[96:111], v[128:131], v[80:83], v[96:111]\n\t"
    "s_waitcnt lgkmcnt(1)\n\t"
    "ds_read_b64_tr_b16 v[128:129], v165 offset:0\n\t"
    "ds_read_b64_tr_b16 v[130:131], v165 offset:2048\n\t"
    "v_add_f32_e32 v161, v153, v161\n\t"
    "v_exp_f32_e32 v157, v125\n\t"
    "v_add_f32_e32 v161, v154, v161\n\t"
    "v_mfma_f32_32x32x16_bf16 v[96:111], v[132:135], v[84:87], v[96:111]\n\t"
    "s_cselect_b32 s71, 0, s71\n\t"
    "ds_read_b64_tr_b16 v[132:133], v165 offset:512\n\t"
    "ds_read_b64_tr_b16 v[134:135], v165 offset:2560\n\t"
    "v_exp_f32_e32 v158, v126\n\t"
    "v_exp_f32_e32 v159, v127\n\t"
    "v_mfma_f32_32x32x16_bf16 v[96:111], v[136:139], v[88:91], v[96:111]\n\t"
    "s_waitcnt lgkmcnt(2)\n\t"
    "ds_read_b64_tr_b16 v[136:137], v165 offset:1024\n\t"
    "ds_read_b64_tr_b16 v[138:139], v165 offset:3072\n\t"
    "v_add_f32_e32 v161, v155, v161\n\t"
    "v_add_f32_e32 v161, v156, v161\n\t"
    "v_add_f32_e32 v161, v157, v161\n\t"
    "v_cvt_pk_bf16_f32 v148, v152, v153\n\t"
    "v_mfma_f32_32x32x16_bf16 v[96:111], v[140:143], v[92:95], v[96:111]\n\t"
    "ds_read_b64_tr_b16 v[140:141], v165 offset:1536\n\t"
    "ds_read_b64_tr_b16 v[142:143], v165 offset:3584\n\t"
    "v_add_f32_e32 v161, v158, v161\n\t"
    "v_add_f32_e32 v161, v159, v161\n\t"
    "v_cmp_lt_f32_e32 vcc, s69, v161\n\t"
    "v_add_u32_e32 v163, s72, v163\n\t"
    ".p2align 5,,4\n\ts_cbranch_vccnz Lfb_%=\n\t"
    "Lfbr2_%=:\n\t"
    "v_mfma_f32_32x32x16_bf16 v[0:15], v[128:131], v[144:147], v[0:15]\n\t"
    "s_waitcnt lgkmcnt(2)\n\t"
    "ds_read_b64_tr_b16 v[128:129], v165 offset:4096\n\t"
    "ds_read_b64_tr_b16 v[130:131], v165 offset:6144\n\t"
    "v_cvt_pk_bf16_f32 v149, v154, v155\n\t"
    "v_cvt_pk_bf16_f32 v150, v156, v157\n\t"
    "v_cvt_pk_bf16_f32 v151, v158, v159\n\t"
    "v_xor_b32_e32 v164, 32, v163\n\t"
    "v_mfma_f32_32x32x16_bf16 v[16:31], v[132:135], v[144:147], v[16:31]\n\t"
    "ds_read_b64_tr_b16 v[132:133], v165 offset:4608\n\t"
    "ds_read_b64_tr_b16 v[134:135], v165 offset:6656\n\t"
    "v_fma_f32 v112, v96, s68, v162\n\t"
    "v_fma_f32 v113, v97, s68, v162\n\t"
    "v_fma_f32 v114, v98, s68, v162\n\t"
    "v_fma_f32 v115, v99, s68, v162\n\t"
    "v_mfma_f32_32x32x16_bf16 v[32:47], v[136:139], v[144:147], v[32:47]\n\t"
    "s_waitcnt lgkmcnt(2)\n\t"
    "ds_read_b64_tr_b16 v[136:137], v165 offset:5120\n\t"
    "ds_read_b64_tr_b16 v[138:139], v165 offset:7168\n\t"
    "v_fma_f32 v116, v100, s68, v162\n\t"
    "v_fma_f32 v117, v101, s68, v162\n\t"
    "v_fma_f32 v118, v102, s68, v162\n\t"
    "v_fma_f32 v119, v103, s68, v162\n\t"
    "v_mfma_f32_32x32x16_bf16 v[48:63], v[140:143], v[144:147], v[48:63]\n\t"
    "ds_read_b64_tr_b16 v[140:141], v165 offset:5632\n\t"
    "ds_read_b64_tr_b16 v[142:143], v165 offset:7680\n\t"
    "v_fma_f32 v120, v104, s68, v162\n\t"
    "v_fma_f32 v121, v105, s68, v162\n\t"
    "v_fma_f32 v122, v106, s68, v162\n\t"
    "v_fma_f32 v123, v107, s68, v162\n\t"
    "v_mfma_f32_32x32x16_bf16 v[0:15], v[128:131], v[148:151], v[0:15]\n\t"
    "s_waitcnt lgkmcnt(2)\n\t"
    "ds_read_b128 v[128:131], v163 offset:0\n\t"
    "v_fma_f32 v124, v108, s68, v162\n\t"
    "v_fma_f32 v125, v109, s68, v162\n\t"
    "v_fma_f32 v126, v110, s68, v162\n\t"
    "v_fma_f32 v127, v111, s68, v162\n\t"
    "v_mfma_f32_32x32x16_bf16 v[16:31], v[132:135], v[148:151], v[16:31]\n\t"
    "Lexit_%=:\n\t"
    "v_add_u32_e32 v165, s73, v165\n\t"
    "v_exp_f32_e32 v152, v112\n\t"
    "v_exp_f32_e32 v153, v113\n\t"
    "v_add_f32_e32 v160, v160, v161\n\t"
    "v_mfma_f32_32x32x16_bf16 v[32:47], v[136:139], v[148:151], v[32:47]\n\t"
    "s_waitcnt lgkmcnt(1)\n\t"
    "ds_read_b64_tr_b16 v[64:65], v165 offset:0\n\t"
    "ds_read_b64_tr_b16 v[66:67], v165 offset:2048\n\t"
    "ds_read_b64_tr_b16 v[68:69], v165 offset:4096\n\t"
    "ds_read_b64_tr_b16 v[70:71], v165 offset:6144\n\t"
    "ds_read_b64_tr_b16 v[72:73], v165 offset:512\n\t"
    "ds_read_b64_tr_b16 v[74:75], v165 offset:2560\n\t"
    "ds_read_b64_tr_b16 v[76:77], v165 offset:4608\n\t"
    "ds_read_b64_tr_b16 v[78:79], v165 offset:6656\n\t"
    "v_exp_f32_e32 v154, v114\n\t"
    "v_exp_f32_e32 v155, v115\n\t"
    "v_add_f32_e32 v161, v152, v153\n\t"
    "v_mfma_f32_32x32x16_bf16 v[48:63], v[140:143], v[148:151], v[48:63]\n\t"
    "ds_read_b64_tr_b16 v[80:81], v165 offset:1024\n\t"
    "ds_read_b64_tr_b16 v[82:83], v165 offset:3072\n\t"
    "ds_read_b64_tr_b16 v[84:85], v165 offset:5120\n\t"
    "ds_read_b64_tr_b16 v[86:87], v165 offset:7168\n\t"
    "ds_read_b64_tr_b16 v[88:89], v165 offset:1536\n\t"
    "ds_read_b64_tr_b16 v[90:91], v165 offset:3584\n\t"
    "ds_read_b64_tr_b16 v[92:93], v165 offset:5632\n\t"
    "ds_read_b64_tr_b16 v[94:95], v165 offset:7680\n\t"
    "v_exp_f32_e32 v156, v116\n\t"
    "v_exp_f32_e32 v157, v117\n\t"
    "v_add_f32_e32 v161, v154, v161\n\t"
    "v_cvt_pk_bf16_f32 v144, v152, v153\n\t"
    "Ldrain_%=:\n\t"
    "v_exp_f32_e32 v158, v118\n\t"
    "v_exp_f32_e32 v159, v119\n\t"
    "v_add_f32_e32 v161, v155, v161\n\t"
    "v_cvt_pk_bf16_f32 v145, v154, v155\n\t"
    "v_exp_f32_e32 v152, v120\n\t"
    "v_add_f32_e32 v161, v156, v161\n\t"
    "v_add_f32_e32 v161, v157, v161\n\t"
    "v_cvt_pk_bf16_f32 v146, v156, v157\n\t"
    "v_exp_f32_e32 v153, v121\n\t"
    "v_exp_f32_e32 v154, v122\n\t"
    "v_add_f32_e32 v161, v158, v161\n\t"
    "v_add_f32_e32 v161, v159, v161\n\t"
    "v_exp_f32_e32 v155, v123\n\t"
    "v_exp_f32_e32 v156, v124\n\t"
    "v_cvt_pk_bf16_f32 v147, v158, v159\n\t"
    "v_add_f32_e32 v161, v152, v161\n\t"
    "v_add_f32_e32 v161, v153, v161\n\t"
    "v_exp_f32_e32 v157, v125\n\t"
    "v_add_f32_e32 v161, v154, v161\n\t"
    "v_exp_f32_e32 v158, v126\n\t"
    "v_exp_f32_e32 v159, v127\n\t"
    "v_add_f32_e32 v161, v155, v161\n\t"
    "v_add_f32_e32 v161, v156, v161\n\t"
    "v_add_f32_e32 v161, v157, v161\n\t"
    "v_cvt_pk_bf16_f32 v148, v152, v153\n\t"
    "v_add_f32_e32 v161, v158, v161\n\t"
    "v_add_f32_e32 v161, v159, v161\n\t"
    "v_cmp_lt_f32_e32 vcc, s69, v161\n\t"
    "s_mov_b32 s89, 1\n\t"
    ".p2align 5,,4\n\ts_cbranch_vccnz Lfb_%=\n\t"
    "Lfbr1_%=:\n\t"
    "s_waitcnt lgkmcnt(0)\n\t"
    "v_mfma_f32_32x32x16_bf16 v[0:15], v[64:67], v[144:147], v[0:15]\n\t"
    "s_mov_b32 m0, s78\n\t"
    "v_cvt_pk_bf16_f32 v149, v154, v155\n\t"
    "v_cvt_pk_bf16_f32 v150, v156, v157\n\t"
    "v_cvt_pk_bf16_f32 v151, v158, v159\n\t"
    "v_add_f32_e32 v160, v160, v161\n\t"
    "v_mov_b32 v161, v160\n\t"
    "v_mfma_f32_32x32x16_bf16 v[0:15], v[68:71], v[148:151], v[0:15]\n\t"
    "s_add_u32 s81, s79, 0x8000\n\t"
    "v_permlane32_swap_b32 v160, v161\n\t"
    "v_and_b32 v97, 7, v166\n\t"
    "v_add_f32_e32 v160, v160, v161\n\t"
    "v_rcp_f32_e32 v160, v160\n\t"
    "v_mfma_f32_32x32x16_bf16 v[16:31], v[72:75], v[144:147], v[16:31]\n\t"
    "s_add_u32 s82, s79, 0x10000\n\t"
    "v_lshrrev_b32 v99, 3, v166\n\t"
    "v_lshlrev_b32 v98, 4, v97\n\t"
    "v_lshl_or_b32 v98, v99, 12, v98\n\t"
    "v_mul_u32_u24 v96, 528, v97\n\t"
    "v_lshl_add_u32 v99, v99, 2, s78\n\t"
    "v_mfma_f32_32x32x16_bf16 v[16:31], v[76:79], v[148:151], v[16:31]\n\t"
    "s_add_u32 s83, s79, 0x18000\n\t"
    "v_add_u32 v96, v99, v96\n\t"
    "v_add_u32 v97, 3968, v96\n\t"
    "v_mul_f32_e32 v0, v160, v0\n\t"
    "v_mul_f32_e32 v1, v160, v1\n\t"
    "v_mul_f32_e32 v2, v160, v2\n\t"
    "v_mfma_f32_32x32x16_bf16 v[32:47], v[80:83], v[144:147], v[32:47]\n\t"
    "v_mul_f32_e32 v3, v160, v3\n\t"
    "v_mul_f32_e32 v4, v160, v4\n\t"
    "v_mul_f32_e32 v5, v160, v5\n\t"
    "v_mul_f32_e32 v6, v160, v6\n\t"
    "v_mul_f32_e32 v7, v160, v7\n\t"
    "v_mfma_f32_32x32x16_bf16 v[32:47], v[84:87], v[148:151], v[32:47]\n\t"
    "v_cvt_pk_bf16_f32 v0, v0, v1\n\t"
    "v_cvt_pk_bf16_f32 v1, v2, v3\n\t"
    "v_cvt_pk_bf16_f32 v2, v4, v5\n\t"
    "v_cvt_pk_bf16_f32 v3, v6, v7\n\t"
    "v_mul_f32_e32 v8, v160, v8\n\t"
    "v_mfma_f32_32x32x16_bf16 v[48:63], v[88:91], v[144:147], v[48:63]\n\t"
    "v_mul_f32_e32 v9, v160, v9\n\t"
    "v_mul_f32_e32 v10, v160, v10\n\t"
    "v_mul_f32_e32 v11, v160, v11\n\t"
    "v_mul_f32_e32 v12, v160, v12\n\t"
    "v_mul_f32_e32 v13, v160, v13\n\t"
    "v_mfma_f32_32x32x16_bf16 v[48:63], v[92:95], v[148:151], v[48:63]\n\t"
    "s_cmp_le_u32 s75, 1\n\t"
    ".p2align 5,,4\n\ts_cbranch_scc1 Lb6_%=\n\t"
    "s_setprio 0\n\t"
    "s_barrier\n\t"
    "s_sub_u32 s75, s75, 1\n\t"
    "Lb6_%=:\n\t"
    "v_mul_f32_e32 v14, v160, v14\n\t"
    "v_mul_f32_e32 v15, v160, v15\n\t"
    "v_cvt_pk_bf16_f32 v8, v8, v9\n\t"
    "v_cvt_pk_bf16_f32 v9, v10, v11\n\t"
    "v_cvt_pk_bf16_f32 v10, v12, v13\n\t"
    "v_cvt_pk_bf16_f32 v11, v14, v15\n\t"
    "ds_write_addtid_b32 v0 offset:0\n\t"
    "ds_write_addtid_b32 v1 offset:264\n\t"
    "ds_write_addtid_b32 v2 offset:528\n\t"
    "ds_write_addtid_b32 v3 offset:792\n\t"
    "ds_write_addtid_b32 v8 offset:1056\n\t"
    "ds_write_addtid_b32 v9 offset:1320\n\t"
    "ds_write_addtid_b32 v10 offset:1584\n\t"
    "ds_write_addtid_b32 v11 offset:1848\n\t"
    "s_cmp_le_u32 s75, 1\n\t"
    ".p2align 5,,4\n\ts_cbranch_scc1 Lb7_%=\n\t"
    "s_barrier\n\t"
    "s_sub_u32 s75, s75, 1\n\t"
    "Lb7_%=:\n\t"
    "v_mul_f32_e32 v16, v160, v16\n\t"
    "v_mul_f32_e32 v17, v160, v17\n\t"
    "v_mul_f32_e32 v18, v160, v18\n\t"
    "v_mul_f32_e32 v19, v160, v19\n\t"
    "v_mul_f32_e32 v20, v160, v20\n\t"
    "v_mul_f32_e32 v21, v160, v21\n\t"
    "v_mul_f32_e32 v22, v160, v22\n\t"
    "v_mul_f32_e32 v23, v160, v23\n\t"
    "v_cvt_pk_bf16_f32 v16, v16, v17\n\t"
    "v_cvt_pk_bf16_f32 v17, v18, v19\n\t"
    "v_cvt_pk_bf16_f32 v18, v20, v21\n\t"
    "v_cvt_pk_bf16_f32 v19, v22, v23\n\t"
    "v_mul_f32_e32 v24, v160, v24\n\t"
    "v_mul_f32_e32 v25, v160, v25\n\t"
    "v_mul_f32_e32 v26, v160, v26\n\t"
    "v_mul_f32_e32 v27, v160, v27\n\t"
    "v_mul_f32_e32 v28, v160, v28\n\t"
    "v_mul_f32_e32 v29, v160, v29\n\t"
    "v_mul_f32_e32 v30, v160, v30\n\t"
    "v_mul_f32_e32 v31, v160, v31\n\t"
    "v_cvt_pk_bf16_f32 v24, v24, v25\n\t"
    "v_cvt_pk_bf16_f32 v25, v26, v27\n\t"
    "v_cvt_pk_bf16_f32 v26, v28, v29\n\t"
    "v_cvt_pk_bf16_f32 v27, v30, v31\n\t"
    "ds_write_addtid_b32 v16 offset:2112\n\t"
    "ds_write_addtid_b32 v17 offset:2376\n\t"
    "ds_write_addtid_b32 v18 offset:2640\n\t"
    "ds_write_addtid_b32 v19 offset:2904\n\t"
    "ds_write_addtid_b32 v24 offset:3168\n\t"
    "ds_write_addtid_b32 v25 offset:3432\n\t"
    "ds_write_addtid_b32 v26 offset:3696\n\t"
    "ds_write_addtid_b32 v27 offset:3960\n\t"
    "ds_read2_b32 v[0:1], v96 offset0:0 offset1:66\n\t"
    "ds_read2_b32 v[2:3], v96 offset0:32 offset1:98\n\t"
    "ds_read2_b32 v[4:5], v96 offset0:8 offset1:74\n\t"
    "ds_read2_b32 v[6:7], v96 offset0:40 offset1:106\n\t"
    "ds_read2_b32 v[8:9], v96 offset0:16 offset1:82\n\t"
    "ds_read2_b32 v[10:11], v96 offset0:48 offset1:114\n\t"
    "ds_read2_b32 v[12:13], v96 offset0:24 offset1:90\n\t"
    "ds_read2_b32 v[14:15], v96 offset0:56 offset1:122\n\t"
    "s_waitcnt lgkmcnt(6)\n\t"
    "buffer_store_dwordx4 v[0:3], v98, s[48:51], s79 offen offset:0 sc1\n\t"
    "s_waitcnt lgkmcnt(4)\n\t"
    "buffer_store_dwordx4 v[4:7], v98, s[48:51], s81 offen offset:0 sc1\n\t"
    "s_waitcnt lgkmcnt(2)\n\t"
    "buffer_store_dwordx4 v[8:11], v98, s[48:51], s82 offen offset:0 sc1\n\t"
    "s_waitcnt lgkmcnt(0)\n\t"
    "buffer_store_dwordx4 v[12:15], v98, s[48:51], s83 offen offset:0 sc1\n\t"
    "s_cmp_le_u32 s75, 1\n\t"
    ".p2align 5,,4\n\ts_cbranch_scc1 Lb8_%=\n\t"
    "s_barrier\n\t"
    "s_sub_u32 s75, s75, 1\n\t"
    "Lb8_%=:\n\t"
    "v_mul_f32_e32 v32, v160, v32\n\t"
    "v_mul_f32_e32 v33, v160, v33\n\t"
    "v_mul_f32_e32 v34, v160, v34\n\t"
    "v_mul_f32_e32 v35, v160, v35\n\t"
    "v_mul_f32_e32 v36, v160, v36\n\t"
    "v_mul_f32_e32 v37, v160, v37\n\t"
    "v_mul_f32_e32 v38, v160, v38\n\t"
    "v_mul_f32_e32 v39, v160, v39\n\t"
    "v_cvt_pk_bf16_f32 v32, v32, v33\n\t"
    "v_cvt_pk_bf16_f32 v33, v34, v35\n\t"
    "v_cvt_pk_bf16_f32 v34, v36, v37\n\t"
    "v_cvt_pk_bf16_f32 v35, v38, v39\n\t"
    "v_mul_f32_e32 v40, v160, v40\n\t"
    "v_mul_f32_e32 v41, v160, v41\n\t"
    "v_mul_f32_e32 v42, v160, v42\n\t"
    "v_mul_f32_e32 v43, v160, v43\n\t"
    "v_mul_f32_e32 v44, v160, v44\n\t"
    "v_mul_f32_e32 v45, v160, v45\n\t"
    "v_mul_f32_e32 v46, v160, v46\n\t"
    "v_mul_f32_e32 v47, v160, v47\n\t"
    "v_cvt_pk_bf16_f32 v40, v40, v41\n\t"
    "v_cvt_pk_bf16_f32 v41, v42, v43\n\t"
    "v_cvt_pk_bf16_f32 v42, v44, v45\n\t"
    "v_cvt_pk_bf16_f32 v43, v46, v47\n\t"
    "ds_write_addtid_b32 v32 offset:3968\n\t"
    "ds_write_addtid_b32 v33 offset:4232\n\t"
    "ds_write_addtid_b32 v34 offset:4496\n\t"
    "ds_write_addtid_b32 v35 offset:4760\n\t"
    "ds_write_addtid_b32 v40 offset:5024\n\t"
    "ds_write_addtid_b32 v41 offset:5288\n\t"
    "ds_write_addtid_b32 v42 offset:5552\n\t"
    "ds_write_addtid_b32 v43 offset:5816\n\t"
    "s_cmp_le_u32 s75, 1\n\t"
    ".p2align 5,,4\n\ts_cbranch_scc1 Lb9_%=\n\t"
    "s_barrier\n\t"
    "s_sub_u32 s75, s75, 1\n\t"
    "Lb9_%=:\n\t"
    "v_mul_f32_e32 v48, v160, v48\n\t"
    "v_mul_f32_e32 v49, v160, v49\n\t"
    "v_mul_f32_e32 v50, v160, v50\n\t"
    "v_mul_f32_e32 v51, v160, v51\n\t"
    "v_mul_f32_e32 v52, v160, v52\n\t"
    "v_mul_f32_e32 v53, v160, v53\n\t"
    "v_mul_f32_e32 v54, v160, v54\n\t"
    "v_mul_f32_e32 v55, v160, v55\n\t"
    "v_cvt_pk_bf16_f32 v48, v48, v49\n\t"
    "v_cvt_pk_bf16_f32 v49, v50, v51\n\t"
    "v_cvt_pk_bf16_f32 v50, v52, v53\n\t"
    "v_cvt_pk_bf16_f32 v51, v54, v55\n\t"
    "v_mul_f32_e32 v56, v160, v56\n\t"
    "v_mul_f32_e32 v57, v160, v57\n\t"
    "v_mul_f32_e32 v58, v160, v58\n\t"
    "v_mul_f32_e32 v59, v160, v59\n\t"
    "v_mul_f32_e32 v60, v160, v60\n\t"
    "v_mul_f32_e32 v61, v160, v61\n\t"
    "v_mul_f32_e32 v62, v160, v62\n\t"
    "v_mul_f32_e32 v63, v160, v63\n\t"
    "v_cvt_pk_bf16_f32 v56, v56, v57\n\t"
    "v_cvt_pk_bf16_f32 v57, v58, v59\n\t"
    "v_cvt_pk_bf16_f32 v58, v60, v61\n\t"
    "v_cvt_pk_bf16_f32 v59, v62, v63\n\t"
    "ds_write_addtid_b32 v48 offset:6080\n\t"
    "ds_write_addtid_b32 v49 offset:6344\n\t"
    "ds_write_addtid_b32 v50 offset:6608\n\t"
    "ds_write_addtid_b32 v51 offset:6872\n\t"
    "ds_write_addtid_b32 v56 offset:7136\n\t"
    "ds_write_addtid_b32 v57 offset:7400\n\t"
    "ds_write_addtid_b32 v58 offset:7664\n\t"
    "ds_write_addtid_b32 v59 offset:7928\n\t"
    "ds_read2_b32 v[16:17], v97 offset0:0 offset1:66\n\t"
    "ds_read2_b32 v[18:19], v97 offset0:32 offset1:98\n\t"
    "ds_read2_b32 v[20:21], v97 offset0:8 offset1:74\n\t"
    "ds_read2_b32 v[22:23], v97 offset0:40 offset1:106\n\t"
    "ds_read2_b32 v[24:25], v97 offset0:16 offset1:82\n\t"
    "ds_read2_b32 v[26:27], v97 offset0:48 offset1:114\n\t"
    "ds_read2_b32 v[28:29], v97 offset0:24 offset1:90\n\t"
    "ds_read2_b32 v[30:31], v97 offset0:56 offset1:122\n\t"
    "s_waitcnt lgkmcnt(6)\n\t"
    "buffer_store_dwordx4 v[16:19], v98, s[48:51], s79 offen offset:128 sc1\n\t"
    "s_waitcnt lgkmcnt(4)\n\t"
    "buffer_store_dwordx4 v[20:23], v98, s[48:51], s81 offen offset:128 sc1\n\t"
    "s_waitcnt lgkmcnt(2)\n\t"
    "buffer_store_dwordx4 v[24:27], v98, s[48:51], s82 offen offset:128 sc1\n\t"
    "s_waitcnt lgkmcnt(0)\n\t"
    "buffer_store_dwordx4 v[28:31], v98, s[48:51], s83 offen offset:128 sc1\n\t"
    "s_sub_u32 s75, s75, s21\n\t"
    "Lowed_%=:\n\t"
    "s_sub_u32 s75, s75, 1\n\t"
    ".p2align 5,,4\n\ts_cbranch_scc1 Lrend_%=\n\t"
    "s_barrier\n\t"
    "s_branch Lowed_%=\n\t"
    "Lsecond_%=:\n\t"
    "s_lshl_b32 s75, s21, 1\n\t"
    "s_mov_b32 s98, 4\n\t"
    "s_sub_u32 s95, s95, 1\n\t"
    "s_cmp_ge_u32 s95, 8\n\t"
    "s_cselect_b32 s81, 1, 0\n\t"
    "s_sub_i32 s80, s95, 6\n\t"
    "s_max_i32 s80, s80, 0\n\t"
    "s_cmp_gt_u32 s55, 4\n\t"
    "s_cselect_b32 s84, s80, 0\n\t"
    "s_cselect_b32 s81, 0, s81\n\t"
    "s_waitcnt vmcnt(0)\n\t"
    "v_mfma_f32_32x32x2_f32 v[96:111], v162, v161, 0\n\t"
    "v_mfma_f32_32x32x16_bf16 v[0:15], v[144:147], v[144:147], 0\n\t"
    "v_mfma_f32_32x32x16_bf16 v[16:31], v[144:147], v[144:147], 0\n\t"
    "v_mfma_f32_32x32x16_bf16 v[32:47], v[144:147], v[144:147], 0\n\t"
    "v_mfma_f32_32x32x16_bf16 v[48:63], v[144:147], v[144:147], 0\n\t"
    "s_barrier\n\t"
    "s_cmp_ge_u32 s76, 4\n\t"
    ".p2align 5,,4\n\ts_cbranch_scc1 Ls10_%=\n\t"
    "s_branch Ls12_%=\n\t"
    "Ls11_%=:\n\t"
    "s_barrier\n\t"
    "s_add_u32 s71, s71, 0x4000\n\t"
    "s_cmp_eq_u32 s71, 98304\n\t"
    "s_cselect_b32 s71, 0, s71\n\t"
    "s_cmp_eq_u32 s95, 0\n\t"
    ".p2align 5,,4\n\ts_cbranch_scc1 Ls13_%=\n\t"
    "s_sub_u32 s84, s84, 1\n\t"
    ".p2align 5,,4\n\ts_cbranch_scc0 Ls12_%=\n\t"
    "s_cmp_eq_u32 s98, 0\n\t"
    ".p2align 5,,4\n\ts_cbranch_scc1 Ls12_%=\n\t"
    "Ls10_%=:\n\t"
    "s_mov_b32 m0, s83\n\t"
    "s_add_u32 s83, s83, 896\n\t"
    "buffer_load_dwordx4 v153, s[36:39], s82 offen nt lds\n\t"
    "s_mov_b32 m0, s83\n\t"
    "s_add_u32 s83, s83, 1152\n\t"
    "buffer_load_dwordx4 v153, s[36:39], s82 offen offset:128 nt lds\n\t"
    "v_xor_b32 v153, 32, v153\n\t"
    "s_add_u32 s82, s82, 0x8000\n\t"
    "s_sub_u32 s98, s98, 1\n\t"
    "s_mov_b32 s84, s81\n\t"
    "Ls12_%=:\n\t"
    "s_sub_u32 s95, s95, 1\n\t"
    "s_branch Ls11_%=\n\t"
    "Ls13_%=:\n\t"
    "s_sub_u32 s98, s98, 1\n\t"
    ".p2align 5,,4\n\ts_cbranch_scc1 Ls14_%=\n\t"
    "s_mov_b32 m0, s83\n\t"
    "s_add_u32 s83, s83, 896\n\t"
    "buffer_load_dwordx4 v153, s[36:39], s82 offen nt lds\n\t"
    "s_mov_b32 m0, s83\n\t"
    "s_add_u32 s83, s83, 1152\n\t"
    "buffer_load_dwordx4 v153, s[36:39], s82 offen offset:128 nt lds\n\t"
    "v_xor_b32 v153, 32, v153\n\t"
    "s_add_u32 s82, s82, 0x8000\n\t"
    "s_branch Ls13_%=\n\t"
    "Ls14_%=:\n\t"
    "s_waitcnt vmcnt(0)\n\t"
    "ds_read_b128 v[64:67], v154 offset:0\n\t"
    "ds_read_b128 v[68:71], v155 offset:0\n\t"
    "ds_read_b128 v[72:75], v154 offset:512\n\t"
    "ds_read_b128 v[76:79], v155 offset:512\n\t"
    "ds_read_b128 v[80:83], v154 offset:1024\n\t"
    "ds_read_b128 v[84:87], v155 offset:1024\n\t"
    "ds_read_b128 v[88:91], v154 offset:1536\n\t"
    "ds_read_b128 v[92:95], v155 offset:1536\n\t"
    "s_add_u32 s71, s71, 0x4000\n\t"
    "s_cmp_eq_u32 s71, 98304\n\t"
    "s_cselect_b32 s71, 0, s71\n\t"
    "v_add_u32 v163, s71, v163\n\t"
    "v_add_u32 v165, s71, v165\n\t"
    "v_xor_b32_e32 v164, 32, v163\n\t"
    "s_waitcnt lgkmcnt(0)\n\t"
    "s_barrier\n\t"
    "ds_read_b128 v[128:131], v163 offset:0\n\t"
    "ds_read_b128 v[132:135], v164 offset:0\n\t"
    "ds_read_b128 v[136:139], v163 offset:512\n\t"
    "ds_read_b128 v[140:143], v164 offset:512\n\t"
    "ds_read_b128 v[112:115], v163 offset:1024\n\t"
    "ds_read_b128 v[116:119], v164 offset:1024\n\t"
    "ds_read_b128 v[120:123], v163 offset:1536\n\t"
    "ds_read_b128 v[124:127], v164 offset:1536\n\t"
    "s_branch Lfirst_%=\n\t"
    "Lhelper_%=:\n\t"
    "s_or_b32 s80, s55, s59\n\t"
    "s_bitcmp1_b32 s80, 3\n\t"
    "s_cselect_b32 s80, 11, 7\n\t"
    "s_cmp_eq_u32 s80, s77\n\t"
    ".p2align 5,,4\n\ts_cbranch_scc1 Lloader_%=\n\t"
    "s_cmp_eq_u32 s26, 0\n\t"
    ".p2align 5,,4\n\ts_cbranch_scc1 Ldone_%=\n\t"
    "s_add_u32 s95, s52, 31\n\t"
    "s_lshr_b32 s95, s95, 5\n\t"
    "s_add_u32 s96, s56, 31\n\t"
    "s_lshr_b32 s96, s96, 5\n\t"
    "s_cmp_eq_u32 s59, 0\n\t"
    "s_cselect_b32 s96, 0, s96\n\t"
    "s_cselect_b32 s3, s55, s59\n\t"
    "s_cmp_gt_u32 s3, 4\n\t"
    "s_cselect_b32 s3, 1, 0\n\t"
    "s_cselect_b32 s21, 0, s96\n\t"
    "s_cmp_ge_u32 s21, 3\n\t"
    "s_cselect_b32 s3, 1, s3\n\t"
    "s_min_u32 s21, s21, 1\n\t"
    "s_sub_u32 s75, s95, s3\n\t"
    "s_add_u32 s75, s75, 1\n\t"
    "s_branch Lowed_%=\n\t"
    "Lloader_%=:\n\t"
    "s_setprio 3\n\t"
    "s_lshl_b32 s84, s52, 12\n\t"
    "s_add_u32 s84, s84, s53\n\t"
    "s_add_u32 s42, s84, 256\n\t"
    "s_mov_b32 s46, s42\n\t"
    "s_sub_u32 s93, s52, 1\n\t"
    "s_lshr_b32 s93, s93, 5\n\t"
    "s_lshl_b32 s84, s93, 17\n\t"
    "s_add_u32 s91, s84, s53\n\t"
    "s_mov_b32 s92, s53\n\t"
    "v_xor_b32 v16, 32, v153\n\t"
    "v_add_u32 v32, 4096, v153\n\t"
    "v_add_u32 v33, 3200, v153\n\t"
    "v_add_u32 v34, 34816, v16\n\t"
    "v_add_u32 v35, 33920, v16\n\t"
    "v_add_u32 v36, 69632, v153\n\t"
    "v_add_u32 v37, 68736, v153\n\t"
    "v_add_u32 v38, 100352, v16\n\t"
    "v_add_u32 v39, 99456, v16\n\t"
    "v_add_u32 v40, 4096, v152\n\t"
    "v_add_u32 v41, 3200, v152\n\t"
    "v_add_u32 v42, 34816, v152\n\t"
    "v_add_u32 v43, 33920, v152\n\t"
    "v_add_u32 v44, 69632, v152\n\t"
    "v_add_u32 v45, 68736, v152\n\t"
    "v_add_u32 v46, 100352, v152\n\t"
    "v_add_u32 v47, 99456, v152\n\t"
    "s_mov_b32 s90, 0\n\t"
    "s_mov_b32 s98, s59\n\t"
    "s_mov_b32 s70, 0\n\t"
    "s_add_u32 m0, s70, 65536\n\t"
    "s_nop 0\n\t"
    "buffer_load_dwordx4 v32, s[40:43], s91 offen offset:0 lds\n\t"
    "buffer_load_dwordx4 v33, s[40:43], s91 offen offset:1024 lds\n\t"
    "buffer_load_dwordx4 v34, s[40:43], s91 offen offset:2048 lds\n\t"
    "buffer_load_dwordx4 v35, s[40:43], s91 offen offset:3072 lds\n\t"
    "s_add_u32 m0, s70, 69632\n\t"
    "s_nop 0\n\t"
    "buffer_load_dwordx4 v36, s[40:43], s91 offen offset:0 lds\n\t"
    "buffer_load_dwordx4 v37, s[40:43], s91 offen offset:1024 lds\n\t"
    "buffer_load_dwordx4 v38, s[40:43], s91 offen offset:2048 lds\n\t"
    "buffer_load_dwordx4 v39, s[40:43], s91 offen offset:3072 lds\n\t"
    "s_add_u32 m0, s70, 73728\n\t"
    "s_nop 0\n\t"
    "buffer_load_dwordx4 v40, s[44:47], s91 offen offset:0 lds\n\t"
    "buffer_load_dwordx4 v41, s[44:47], s91 offen offset:1024 lds\n\t"
    "buffer_load_dwordx4 v42, s[44:47], s91 offen offset:2048 lds\n\t"
    "buffer_load_dwordx4 v43, s[44:47], s91 offen offset:3072 lds\n\t"
    "s_add_u32 m0, s70, 77824\n\t"
    "s_sub_u32 s93, s93, 1\n\t"
    "buffer_load_dwordx4 v44, s[44:47], s91 offen offset:0 lds\n\t"
    "buffer_load_dwordx4 v45, s[44:47], s91 offen offset:1024 lds\n\t"
    "buffer_load_dwordx4 v46, s[44:47], s91 offen offset:2048 lds\n\t"
    "buffer_load_dwordx4 v47, s[44:47], s91 offen offset:3072 lds\n\t"
    ".p2align 5,,4\n\ts_cbranch_scc1 Le17_%=\n\t"
    "s_mov_b32 s91, s92\n\t"
    "s_add_u32 s92, s92, 0x20000\n\t"
    "Le16_%=:\n\t"
    "s_mov_b32 s70, 0x4000\n\t"
    "s_add_u32 m0, s70, 65536\n\t"
    "s_nop 0\n\t"
    "buffer_load_dwordx4 v32, s[40:43], s91 offen offset:0 lds\n\t"
    "buffer_load_dwordx4 v33, s[40:43], s91 offen offset:1024 lds\n\t"
    "buffer_load_dwordx4 v34, s[40:43], s91 offen offset:2048 lds\n\t"
    "buffer_load_dwordx4 v35, s[40:43], s91 offen offset:3072 lds\n\t"
    "s_add_u32 m0, s70, 69632\n\t"
    "s_nop 0\n\t"
    "buffer_load_dwordx4 v36, s[40:43], s91 offen offset:0 lds\n\t"
    "buffer_load_dwordx4 v37, s[40:43], s91 offen offset:1024 lds\n\t"
    "buffer_load_dwordx4 v38, s[40:43], s91 offen offset:2048 lds\n\t"
    "buffer_load_dwordx4 v39, s[40:43], s91 offen offset:3072 lds\n\t"
    "s_add_u32 s95, s52, 31\n\t"
    "s_lshr_b32 s95, s95, 5\n\t"
    "s_add_u32 s96, s56, 31\n\t"
    "s_lshr_b32 s96, s96, 5\n\t"
    "s_cmp_eq_u32 s59, 0\n\t"
    "s_cselect_b32 s96, 0, s96\n\t"
    "s_cselect_b32 s3, s55, s59\n\t"
    "s_cmp_gt_u32 s3, 4\n\t"
    "s_cselect_b32 s3, 1, 0\n\t"
    "s_cselect_b32 s21, 0, s96\n\t"
    "s_cmp_ge_u32 s21, 3\n\t"
    "s_cselect_b32 s3, 1, s3\n\t"
    "s_min_u32 s21, s21, 1\n\t"
    "s_add_u32 s74, s95, s96\n\t"
    "s_sub_u32 s74, s74, s3\n\t"
    "s_sub_u32 s74, s74, 1\n\t"
    "s_cmp_le_u32 s55, 4\n\t"
    "s_cselect_b32 s80, 38, 64\n\t"
    "s_cselect_b32 s82, 45, 55\n\t"
    "s_cselect_b32 s83, 28, 42\n\t"
    "s_mul_i32 s80, s80, s95\n\t"
    "s_cmp_le_u32 s59, 4\n\t"
    "s_cselect_b32 s81, 38, 64\n\t"
    "s_mul_i32 s81, s81, s96\n\t"
    "s_add_u32 s80, s80, s81\n\t"
    "s_add_u32 s80, s80, s82\n\t"
    "s_cmp_eq_u32 s59, 8\n\t"
    "s_cselect_b32 s81, 40, 0\n\t"
    "s_add_u32 s80, s80, s81\n\t"
    "s_lshl_b32 s81, s94, 6\n\t"
    "s_sub_i32 s80, s81, s80\n\t"
    "s_mul_i32 s80, s80, s83\n\t"
    "s_ashr_i32 s80, s80, 8\n\t"
    "s_max_i32 s80, s80, -16\n\t"
    "s_min_i32 s80, s80, 8\n\t"
    "s_sub_i32 s81, s94, s95\n\t"
    "s_max_i32 s81, s81, 0\n\t"
    "s_min_i32 s81, s81, 4\n\t"
    "s_mul_i32 s81, s81, 60\n\t"
    "s_mov_b32 s72, 98\n\t"
    "s_cmp_lg_u32 s59, 0\n\t"
    "s_cselect_b32 s72, 100, s72\n\t"
    "s_cselect_b32 s80, s80, 0\n\t"
    "s_cselect_b32 s73, 0, s81\n\t"
    "s_cmp_le_u32 s55, 4\n\t"
    "s_cselect_b32 s72, 78, s72\n\t"
    "s_cselect_b32 s96, 10, 7\n\t"
    "s_cselect_b32 s71, 6, 7\n\t"
    "s_add_i32 s72, s72, s80\n\t"
    "s_cmp_eq_u32 s94, 0\n\t"
    "s_cselect_b32 s96, 0, s96\n\t"
    "s_waitcnt vmcnt(16)\n\t"
    "s_barrier\n\t"
    "s_cmp_lg_u32 s90, 0\n\t"
    ".p2align 5,,4\n\ts_cbranch_scc1 Le18_%=\n\t"
    "s_add_u32 m0, s70, 73728\n\t"
    "s_nop 0\n\t"
    "buffer_load_dwordx4 v40, s[44:47], s91 offen offset:0 lds\n\t"
    "buffer_load_dwordx4 v41, s[44:47], s91 offen offset:1024 lds\n\t"
    "buffer_load_dwordx4 v42, s[44:47], s91 offen offset:2048 lds\n\t"
    "buffer_load_dwordx4 v43, s[44:47], s91 offen offset:3072 lds\n\t"
    "s_add_u32 m0, s70, 77824\n\t"
    "s_sub_u32 s93, s93, 1\n\t"
    "buffer_load_dwordx4 v44, s[44:47], s91 offen offset:0 lds\n\t"
    "buffer_load_dwordx4 v45, s[44:47], s91 offen offset:1024 lds\n\t"
    "buffer_load_dwordx4 v46, s[44:47], s91 offen offset:2048 lds\n\t"
    "buffer_load_dwordx4 v47, s[44:47], s91 offen offset:3072 lds\n\t"
    ".p2align 5,,4\n\ts_cbranch_scc1 Le20_%=\n\t"
    "s_mov_b32 s91, s92\n\t"
    "s_add_u32 s92, s92, 0x20000\n\t"
    "Le19_%=:\n\t"
    "s_mov_b32 s70, 0x8000\n\t"
    "s_cmp_lg_u32 s90, 0\n\t"
    ".p2align 5,,4\n\ts_cbranch_scc1 Le21_%=\n\t"
    "s_add_u32 m0, s70, 65536\n\t"
    "s_nop 0\n\t"
    "buffer_load_dwordx4 v32, s[40:43], s91 offen offset:0 lds\n\t"
    "buffer_load_dwordx4 v33, s[40:43], s91 offen offset:1024 lds\n\t"
    "buffer_load_dwordx4 v34, s[40:43], s91 offen offset:2048 lds\n\t"
    "buffer_load_dwordx4 v35, s[40:43], s91 offen offset:3072 lds\n\t"
    "s_add_u32 m0, s70, 69632\n\t"
    "s_nop 0\n\t"
    "buffer_load_dwordx4 v36, s[40:43], s91 offen offset:0 lds\n\t"
    "buffer_load_dwordx4 v37, s[40:43], s91 offen offset:1024 lds\n\t"
    "buffer_load_dwordx4 v38, s[40:43], s91 offen offset:2048 lds\n\t"
    "buffer_load_dwordx4 v39, s[40:43], s91 offen offset:3072 lds\n\t"
    "s_add_u32 m0, s70, 73728\n\t"
    "s_nop 0\n\t"
    "buffer_load_dwordx4 v40, s[44:47], s91 offen offset:0 lds\n\t"
    "buffer_load_dwordx4 v41, s[44:47], s91 offen offset:1024 lds\n\t"
    "buffer_load_dwordx4 v42, s[44:47], s91 offen offset:2048 lds\n\t"
    "buffer_load_dwordx4 v43, s[44:47], s91 offen offset:3072 lds\n\t"
    "s_add_u32 m0, s70, 77824\n\t"
    "s_sub_u32 s93, s93, 1\n\t"
    "buffer_load_dwordx4 v44, s[44:47], s91 offen offset:0 lds\n\t"
    "buffer_load_dwordx4 v45, s[44:47], s91 offen offset:1024 lds\n\t"
    "buffer_load_dwordx4 v46, s[44:47], s91 offen offset:2048 lds\n\t"
    "buffer_load_dwordx4 v47, s[44:47], s91 offen offset:3072 lds\n\t"
    ".p2align 5,,4\n\ts_cbranch_scc1 Le23_%=\n\t"
    "s_mov_b32 s91, s92\n\t"
    "s_add_u32 s92, s92, 0x20000\n\t"
    "Le22_%=:\n\t"
    "s_waitcnt vmcnt(24)\n\t"
    "s_cmp_le_u32 s90, 1\n\t"
    ".p2align 5,,4\n\ts_cbranch_scc1 Lh24_%=\n\t"
    "s_waitcnt vmcnt(0)\n\t"
    "Lh24_%=:\n\t"
    "s_cmp_eq_u32 s96, 0\n\t"
    ".p2align 5,,4\n\ts_cbranch_scc1 Lh25_%=\n\t"
    "s_memrealtime s[6:7]\n\t"
    "s_waitcnt lgkmcnt(0)\n\t"
    "s_add_u32 s73, s73, s6\n\t"
    "s_sub_u32 s96, s96, 1\n\t"
    "Lt27_%=:\n\t"
    "s_memrealtime s[6:7]\n\t"
    "s_waitcnt lgkmcnt(0)\n\t"
    "s_sub_u32 s6, s6, s73\n\t"
    "s_cmp_ge_i32 s6, 0\n\t"
    ".p2align 5,,4\n\ts_cbranch_scc1 Lt28_%=\n\t"
    "s_sleep 1\n\t"
    "s_branch Lt27_%=\n\t"
    "Lt28_%=:\n\t"
    "s_add_u32 s73, s73, s72\n\t"
    "s_sub_u32 s72, s72, s71\n\t"
    "Lh25_%=:\n\t"
    "s_barrier\n\t"
    "s_mov_b32 s70, 0xc000\n\t"
    "s_cmp_lg_u32 s90, 0\n\t"
    ".p2align 5,,4\n\ts_cbranch_scc1 Le29_%=\n\t"
    "s_add_u32 m0, s70, 65536\n\t"
    "s_nop 0\n\t"
    "buffer_load_dwordx4 v32, s[40:43], s91 offen offset:0 lds\n\t"
    "buffer_load_dwordx4 v33, s[40:43], s91 offen offset:1024 lds\n\t"
    "buffer_load_dwordx4 v34, s[40:43], s91 offen offset:2048 lds\n\t"
    "buffer_load_dwordx4 v35, s[40:43], s91 offen offset:3072 lds\n\t"
    "s_add_u32 m0, s70, 69632\n\t"
    "s_nop 0\n\t"
    "buffer_load_dwordx4 v36, s[40:43], s91 offen offset:0 lds\n\t"
    "buffer_load_dwordx4 v37, s[40:43], s91 offen offset:1024 lds\n\t"
    "buffer_load_dwordx4 v38, s[40:43], s91 offen offset:2048 lds\n\t"
    "buffer_load_dwordx4 v39, s[40:43], s91 offen offset:3072 lds\n\t"
    "s_add_u32 m0, s70, 73728\n\t"
    "s_nop 0\n\t"
    "buffer_load_dwordx4 v40, s[44:47], s91 offen offset:0 lds\n\t"
    "buffer_load_dwordx4 v41, s[44:47], s91 offen offset:1024 lds\n\t"
    "buffer_load_dwordx4 v42, s[44:47], s91 offen offset:2048 lds\n\t"
    "buffer_load_dwordx4 v43, s[44:47], s91 offen offset:3072 lds\n\t"
    "s_add_u32 m0, s70, 77824\n\t"
    "s_sub_u32 s93, s93, 1\n\t"
    "buffer_load_dwordx4 v44, s[44:47], s91 offen offset:0 lds\n\t"
    "buffer_load_dwordx4 v45, s[44:47], s91 offen offset:1024 lds\n\t"
    "buffer_load_dwordx4 v46, s[44:47], s91 offen offset:2048 lds\n\t"
    "buffer_load_dwordx4 v47, s[44:47], s91 offen offset:3072 lds\n\t"
    ".p2align 5,,4\n\ts_cbranch_scc1 Le31_%=\n\t"
    "s_mov_b32 s91, s92\n\t"
    "s_add_u32 s92, s92, 0x20000\n\t"
    "Le30_%=:\n\t"
    "s_branch Lh26_%=\n\t"
    ".p2align 6\n\t"
    "Lh32_%=:\n\t"
    "s_cmp_lg_u32 s90, 0\n\t"
    ".p2align 5,,4\n\ts_cbranch_scc1 Lh33_%=\n\t"
    "s_waitcnt vmcnt(40)\n\t"
    "Lh34_%=:\n\t"
    "s_cmp_lg_u32 s96, 0\n\t"
    ".p2align 5,,4\n\ts_cbranch_scc1 Lh35_%=\n\t"
    "Lh36_%=:\n\t"
    "s_barrier\n\t"
    "Lh26_%=:\n\t"
    "s_add_u32 s70, s70, 0x4000\n\t"
    "s_cmp_eq_u32 s70, 98304\n\t"
    "s_cselect_b32 s70, 0, s70\n\t"
    "s_cmp_lg_u32 s90, 0\n\t"
    ".p2align 5,,4\n\ts_cbranch_scc1 Le39_%=\n\t"
    "s_add_u32 m0, s70, 65536\n\t"
    "s_nop 0\n\t"
    "buffer_load_dwordx4 v32, s[40:43], s91 offen offset:0 lds\n\t"
    "buffer_load_dwordx4 v33, s[40:43], s91 offen offset:1024 lds\n\t"
    "buffer_load_dwordx4 v34, s[40:43], s91 offen offset:2048 lds\n\t"
    "buffer_load_dwordx4 v35, s[40:43], s91 offen offset:3072 lds\n\t"
    "s_add_u32 m0, s70, 69632\n\t"
    "s_nop 0\n\t"
    "buffer_load_dwordx4 v36, s[40:43], s91 offen offset:0 lds\n\t"
    "buffer_load_dwordx4 v37, s[40:43], s91 offen offset:1024 lds\n\t"
    "buffer_load_dwordx4 v38, s[40:43], s91 offen offset:2048 lds\n\t"
    "buffer_load_dwordx4 v39, s[40:43], s91 offen offset:3072 lds\n\t"
    "s_add_u32 m0, s70, 73728\n\t"
    "s_nop 0\n\t"
    "buffer_load_dwordx4 v40, s[44:47], s91 offen offset:0 lds\n\t"
    "buffer_load_dwordx4 v41, s[44:47], s91 offen offset:1024 lds\n\t"
    "buffer_load_dwordx4 v42, s[44:47], s91 offen offset:2048 lds\n\t"
    "buffer_load_dwordx4 v43, s[44:47], s91 offen offset:3072 lds\n\t"
    "s_add_u32 m0, s70, 77824\n\t"
    "s_sub_u32 s93, s93, 1\n\t"
    "buffer_load_dwordx4 v44, s[44:47], s91 offen offset:0 lds\n\t"
    "buffer_load_dwordx4 v45, s[44:47], s91 offen offset:1024 lds\n\t"
    "buffer_load_dwordx4 v46, s[44:47], s91 offen offset:2048 lds\n\t"
    "buffer_load_dwordx4 v47, s[44:47], s91 offen offset:3072 lds\n\t"
    ".p2align 5,,4\n\ts_cbranch_scc1 Le41_%=\n\t"
    "s_mov_b32 s91, s92\n\t"
    "s_add_u32 s92, s92, 0x20000\n\t"
    "Le40_%=:\n\t"
    "s_sub_u32 s74, s74, 1\n\t"
    ".p2align 5,,4\n\ts_cbranch_scc0 Lh32_%=\n\t"
    "Lrend_%=:\n\t"
    "s_cmp_eq_u32 s26, 0\n\t"
    ".p2align 5,,4\n\ts_cbranch_scc1 Ldone_%=\n\t"
    "s_branch Lnext_%=\n\t"
    "Lm3_%=:\n\t"
    "s_mov_b64 s[60:61], s[56:57]\n\t"
    "s_mov_b64 s[62:63], s[58:59]\n\t"
    "s_min_u32 s26, s59, 1\n\t"
    "s_mov_b32 s59, 0\n\t"
    "s_mov_b32 s94, 0\n\t"
    "s_branch Lround_%=\n\t"
    "Ld5_%=:\n\t"
    "ds_read_b64_tr_b16 v[64:65], v165 offset:0\n\t"
    "ds_read_b64_tr_b16 v[66:67], v165 offset:2048\n\t"
    "ds_read_b64_tr_b16 v[68:69], v165 offset:4096\n\t"
    "ds_read_b64_tr_b16 v[70:71], v165 offset:6144\n\t"
    "ds_read_b64_tr_b16 v[72:73], v165 offset:512\n\t"
    "ds_read_b64_tr_b16 v[74:75], v165 offset:2560\n\t"
    "ds_read_b64_tr_b16 v[76:77], v165 offset:4608\n\t"
    "ds_read_b64_tr_b16 v[78:79], v165 offset:6656\n\t"
    "ds_read_b64_tr_b16 v[80:81], v165 offset:1024\n\t"
    "ds_read_b64_tr_b16 v[82:83], v165 offset:3072\n\t"
    "ds_read_b64_tr_b16 v[84:85], v165 offset:5120\n\t"
    "ds_read_b64_tr_b16 v[86:87], v165 offset:7168\n\t"
    "ds_read_b64_tr_b16 v[88:89], v165 offset:1536\n\t"
    "ds_read_b64_tr_b16 v[90:91], v165 offset:3584\n\t"
    "ds_read_b64_tr_b16 v[92:93], v165 offset:5632\n\t"
    "ds_read_b64_tr_b16 v[94:95], v165 offset:7680\n\t"
    "s_branch Ldrain_%=\n\t"
    "Le17_%=:\n\t"
    "s_call_b64 s[4:5], Lnextjob_%=\n\t"
    "s_branch Le16_%=\n\t"
    "Le18_%=:\n\t"
    "s_add_u32 s90, s90, 1\n\t"
    "s_branch Le19_%=\n\t"
    "Le20_%=:\n\t"
    "s_call_b64 s[4:5], Lnextjob_%=\n\t"
    "s_branch Le19_%=\n\t"
    "Le21_%=:\n\t"
    "s_add_u32 s90, s90, 1\n\t"
    "s_branch Le22_%=\n\t"
    "Le23_%=:\n\t"
    "s_call_b64 s[4:5], Lnextjob_%=\n\t"
    "s_branch Le22_%=\n\t"
    "Le29_%=:\n\t"
    "s_add_u32 s90, s90, 1\n\t"
    "s_branch Le30_%=\n\t"
    "Le31_%=:\n\t"
    "s_call_b64 s[4:5], Lnextjob_%=\n\t"
    "s_branch Le30_%=\n\t"
    "Lh33_%=:\n\t"
    "s_cmp_eq_u32 s74, 0\n\t"
    "s_cselect_b32 s90, 9, s90\n\t"
    "s_waitcnt vmcnt(40)\n\t"
    "s_cmp_le_u32 s90, 1\n\t"
    ".p2align 5,,4\n\ts_cbranch_scc1 Lh34_%=\n\t"
    "s_waitcnt vmcnt(24)\n\t"
    "s_cmp_le_u32 s90, 2\n\t"
    ".p2align 5,,4\n\ts_cbranch_scc1 Lh34_%=\n\t"
    "s_waitcnt vmcnt(8)\n\t"
    "s_cmp_le_u32 s90, 3\n\t"
    ".p2align 5,,4\n\ts_cbranch_scc1 Lh34_%=\n\t"
    "s_waitcnt vmcnt(0)\n\t"
    "s_branch Lh34_%=\n\t"
    "Lh35_%=:\n\t"
    "s_sub_u32 s96, s96, 1\n\t"
    "Lt37_%=:\n\t"
    "s_memrealtime s[6:7]\n\t"
    "s_waitcnt lgkmcnt(0)\n\t"
    "s_sub_u32 s6, s6, s73\n\t"
    "s_cmp_ge_i32 s6, 0\n\t"
    ".p2align 5,,4\n\ts_cbranch_scc1 Lt38_%=\n\t"
    "s_sleep 1\n\t"
    "s_branch Lt37_%=\n\t"
    "Lt38_%=:\n\t"
    "s_add_u32 s73, s73, s72\n\t"
    "s_sub_u32 s72, s72, s71\n\t"
    "s_branch Lh36_%=\n\t"
    "Le39_%=:\n\t"
    "s_add_u32 s90, s90, 1\n\t"
    "s_branch Le40_%=\n\t"
    "Le41_%=:\n\t"
    "s_call_b64 s[4:5], Lnextjob_%=\n\t"
    "s_branch Le40_%=\n\t"
    "Lnextjob_%=:\n\t"
    "s_cmp_eq_u32 s98, 0\n\t"
    ".p2align 5,,4\n\ts_cbranch_scc1 Ln42_%=\n\t"
    "s_mov_b32 s98, 0\n\t"
    "s_lshl_b32 s84, s56, 12\n\t"
    "s_add_u32 s84, s84, s57\n\t"
    "s_add_u32 s42, s84, 256\n\t"
    "s_mov_b32 s46, s42\n\t"
    "s_sub_u32 s93, s56, 1\n\t"
    "s_lshr_b32 s93, s93, 5\n\t"
    "s_lshl_b32 s84, s93, 17\n\t"
    "s_add_u32 s91, s84, s57\n\t"
    "s_mov_b32 s92, s57\n\t"
    "s_setpc_b64 s[4:5]\n\t"
    "Ln42_%=:\n\t"
    "s_mov_b32 s42, 0\n\t"
    "s_mov_b32 s91, 0\n\t"
    "s_mov_b32 s90, 1\n\t"
    "s_setpc_b64 s[4:5]\n\t"
    "Lcold_%=:\n\t"
    "Lfb_%=:\n\t"
    "s_nop 7\n\t"
    "s_nop 7\n\t"
    "v_max3_f32 v152, v112, v113, v114\n\t"
    "v_max3_f32 v152, v152, v115, v116\n\t"
    "v_max3_f32 v152, v152, v117, v118\n\t"
    "v_max3_f32 v152, v152, v119, v120\n\t"
    "v_max3_f32 v152, v152, v121, v122\n\t"
    "v_max3_f32 v152, v152, v123, v124\n\t"
    "v_max3_f32 v152, v152, v125, v126\n\t"
    "v_max3_f32 v152, v152, v127, 0\n\t"
    "v_mov_b32 v153, v152\n\t"
    "s_nop 1\n\t"
    "v_permlane32_swap_b32 v152, v153\n\t"
    "s_nop 1\n\t"
    "v_max_f32_e32 v152, v152, v153\n\t"
    "v_exp_f32_e64 v153, -v152\n\t"
    "v_sub_f32_e32 v162, v162, v152\n\t"
    "v_sub_f32_e32 v112, v112, v152\n\t"
    "v_sub_f32_e32 v113, v113, v152\n\t"
    "v_sub_f32_e32 v114, v114, v152\n\t"
    "v_sub_f32_e32 v115, v115, v152\n\t"
    "v_sub_f32_e32 v116, v116, v152\n\t"
    "v_sub_f32_e32 v117, v117, v152\n\t"
    "v_sub_f32_e32 v118, v118, v152\n\t"
    "v_sub_f32_e32 v119, v119, v152\n\t"
    "v_sub_f32_e32 v120, v120, v152\n\t"
    "v_sub_f32_e32 v121, v121, v152\n\t"
    "v_sub_f32_e32 v122, v122, v152\n\t"
    "v_sub_f32_e32 v123, v123, v152\n\t"
    "v_sub_f32_e32 v124, v124, v152\n\t"
    "v_sub_f32_e32 v125, v125, v152\n\t"
    "v_sub_f32_e32 v126, v126, v152\n\t"
    "v_sub_f32_e32 v127, v127, v152\n\t"
    "v_mul_f32_e32 v160, v153, v160\n\t"
    "v_mul_f32_e32 v0, v153, v0\n\t"
    "v_mul_f32_e32 v1, v153, v1\n\t"
    "v_mul_f32_e32 v2, v153, v2\n\t"
    "v_mul_f32_e32 v3, v153, v3\n\t"
    "v_mul_f32_e32 v4, v153, v4\n\t"
    "v_mul_f32_e32 v5, v153, v5\n\t"
    "v_mul_f32_e32 v6, v153, v6\n\t"
    "v_mul_f32_e32 v7, v153, v7\n\t"
    "v_mul_f32_e32 v8, v153, v8\n\t"
    "v_mul_f32_e32 v9, v153, v9\n\t"
    "v_mul_f32_e32 v10, v153, v10\n\t"
    "v_mul_f32_e32 v11, v153, v11\n\t"
    "v_mul_f32_e32 v12, v153, v12\n\t"
    "v_mul_f32_e32 v13, v153, v13\n\t"
    "v_mul_f32_e32 v14, v153, v14\n\t"
    "v_mul_f32_e32 v15, v153, v15\n\t"
    "v_mul_f32_e32 v16, v153, v16\n\t"
    "v_mul_f32_e32 v17, v153, v17\n\t"
    "v_mul_f32_e32 v18, v153, v18\n\t"
    "v_mul_f32_e32 v19, v153, v19\n\t"
    "v_mul_f32_e32 v20, v153, v20\n\t"
    "v_mul_f32_e32 v21, v153, v21\n\t"
    "v_mul_f32_e32 v22, v153, v22\n\t"
    "v_mul_f32_e32 v23, v153, v23\n\t"
    "v_mul_f32_e32 v24, v153, v24\n\t"
    "v_mul_f32_e32 v25, v153, v25\n\t"
    "v_mul_f32_e32 v26, v153, v26\n\t"
    "v_mul_f32_e32 v27, v153, v27\n\t"
    "v_mul_f32_e32 v28, v153, v28\n\t"
    "v_mul_f32_e32 v29, v153, v29\n\t"
    "v_mul_f32_e32 v30, v153, v30\n\t"
    "v_mul_f32_e32 v31, v153, v31\n\t"
    "v_mul_f32_e32 v32, v153, v32\n\t"
    "v_mul_f32_e32 v33, v153, v33\n\t"
    "v_mul_f32_e32 v34, v153, v34\n\t"
    "v_mul_f32_e32 v35, v153, v35\n\t"
    "v_mul_f32_e32 v36, v153, v36\n\t"
    "v_mul_f32_e32 v37, v153, v37\n\t"
    "v_mul_f32_e32 v38, v153, v38\n\t"
    "v_mul_f32_e32 v39, v153, v39\n\t"
    "v_mul_f32_e32 v40, v153, v40\n\t"
    "v_mul_f32_e32 v41, v153, v41\n\t"
    "v_mul_f32_e32 v42, v153, v42\n\t"
    "v_mul_f32_e32 v43, v153, v43\n\t"
    "v_mul_f32_e32 v44, v153, v44\n\t"
    "v_mul_f32_e32 v45, v153, v45\n\t"
    "v_mul_f32_e32 v46, v153, v46\n\t"
    "v_mul_f32_e32 v47, v153, v47\n\t"
    "v_mul_f32_e32 v48, v153, v48\n\t"
    "v_mul_f32_e32 v49, v153, v49\n\t"
    "v_mul_f32_e32 v50, v153, v50\n\t"
    "v_mul_f32_e32 v51, v153, v51\n\t"
    "v_mul_f32_e32 v52, v153, v52\n\t"
    "v_mul_f32_e32 v53, v153, v53\n\t"
    "v_mul_f32_e32 v54, v153, v54\n\t"
    "v_mul_f32_e32 v55, v153, v55\n\t"
    "v_mul_f32_e32 v56, v153, v56\n\t"
    "v_mul_f32_e32 v57, v153, v57\n\t"
    "v_mul_f32_e32 v58, v153, v58\n\t"
    "v_mul_f32_e32 v59, v153, v59\n\t"
    "v_mul_f32_e32 v60, v153, v60\n\t"
    "v_mul_f32_e32 v61, v153, v61\n\t"
    "v_mul_f32_e32 v62, v153, v62\n\t"
    "v_mul_f32_e32 v63, v153, v63\n\t"
    "v_exp_f32_e32 v152, v112\n\t"
    "v_exp_f32_e32 v153, v113\n\t"
    "s_nop 0\n\t"
    "v_add_f32_e32 v161, v152, v153\n\t"
    "v_cvt_pk_bf16_f32 v144, v152, v153\n\t"
    "v_exp_f32_e32 v154, v114\n\t"
    "v_exp_f32_e32 v155, v115\n\t"
    "s_nop 0\n\t"
    "v_add_f32_e32 v161, v154, v161\n\t"
    "v_add_f32_e32 v161, v155, v161\n\t"
    "v_cvt_pk_bf16_f32 v145, v154, v155\n\t"
    "v_exp_f32_e32 v156, v116\n\t"
    "v_exp_f32_e32 v157, v117\n\t"
    "s_nop 0\n\t"
    "v_add_f32_e32 v161, v156, v161\n\t"
    "v_add_f32_e32 v161, v157, v161\n\t"
    "v_cvt_pk_bf16_f32 v146, v156, v157\n\t"
    "v_exp_f32_e32 v158, v118\n\t"
    "v_exp_f32_e32 v159, v119\n\t"
    "s_nop 0\n\t"
    "v_add_f32_e32 v161, v158, v161\n\t"
    "v_add_f32_e32 v161, v159, v161\n\t"
    "v_cvt_pk_bf16_f32 v147, v158, v159\n\t"
    "v_exp_f32_e32 v152, v120\n\t"
    "v_exp_f32_e32 v153, v121\n\t"
    "s_nop 0\n\t"
    "v_add_f32_e32 v161, v152, v161\n\t"
    "v_add_f32_e32 v161, v153, v161\n\t"
    "v_cvt_pk_bf16_f32 v148, v152, v153\n\t"
    "v_exp_f32_e32 v154, v122\n\t"
    "v_exp_f32_e32 v155, v123\n\t"
    "s_nop 0\n\t"
    "v_add_f32_e32 v161, v154, v161\n\t"
    "v_add_f32_e32 v161, v155, v161\n\t"
    "v_exp_f32_e32 v156, v124\n\t"
    "v_exp_f32_e32 v157, v125\n\t"
    "s_nop 0\n\t"
    "v_add_f32_e32 v161, v156, v161\n\t"
    "v_add_f32_e32 v161, v157, v161\n\t"
    "v_exp_f32_e32 v158, v126\n\t"
    "v_exp_f32_e32 v159, v127\n\t"
    "s_nop 0\n\t"
    "v_add_f32_e32 v161, v158, v161\n\t"
    "v_add_f32_e32 v161, v159, v161\n\t"
    "s_nop 1\n\t"
    "s_cmp_eq_u32 s89, 0\n\t"
    ".p2align 5,,4\n\ts_cbranch_scc1 Lfbr0_%=\n\t"
    "s_cmp_eq_u32 s89, 2\n\t"
    ".p2align 5,,4\n\ts_cbranch_scc1 Lfbr2_%=\n\t"
    "s_branch Lfbr1_%=\n\t"
    "Lnext_%=:\n\t"
    "s_setprio 0\n\t"
    "s_waitcnt vmcnt(0) lgkmcnt(0)\n\t"
    "s_barrier\n\t"
    "v_bfe_u32 v152, v166, 2, 3\n\t"
    "v_lshrrev_b32 v1, 5, v166\n\t"
    "v_lshlrev_b32 v152, 12, v152\n\t"
    "v_lshl_or_b32 v152, v1, 6, v152\n\t"
    "v_and_b32 v1, 3, v166\n\t"
    "v_lshl_or_b32 v152, v1, 4, v152\n\t"
    "v_and_b32 v1, 16, v166\n\t"
    "v_xor_b32 v153, v1, v152\n\t"
    "s_cmp_eq_u32 s26, 2\n\t"
    ".p2align 5,,4\n\ts_cbranch_scc1 Lg43_%=\n\t"
    "s_mov_b64 s[52:53], s[60:61]\n\t"
    "s_mov_b64 s[54:55], s[62:63]\n\t"
    "s_mov_b32 s26, 0\n\t"
    "s_branch Lround_%=\n\t"
    "Lgeneric_%=:\n\t"
    "s_waitcnt vmcnt(0)\n\t"
    "s_mov_b32 s26, 2\n\t"
    "s_sub_u32 s27, s24, 16\n\t"
    "s_mov_b32 s34, 0\n\t"
    "s_mov_b32 s35, 0\n\t"
    "s_mov_b32 s59, 0\n\t"
    "s_mov_b32 s56, 0\n\t"
    "s_mov_b32 s94, 0\n\t"
    "Lg43_%=:\n\t"
    "s_add_u32 s27, s27, 16\n\t"
    "Lg44_%=:\n\t"
    "s_cmp_ge_u32 s34, s20\n\t"
    ".p2align 5,,4\n\ts_cbranch_scc1 Ldone_%=\n\t"
    "s_lshl_b32 s80, s34, 2\n\t"
    "s_add_u32 s4, s16, s80\n\t"
    "s_addc_u32 s5, s17, 0\n\t"
    "s_load_dword s53, s[4:5], 0x0\n\t"
    "s_load_dword s52, s[4:5], 0x4\n\t"
    "s_waitcnt lgkmcnt(0)\n\t"
    "s_sub_u32 s52, s52, s53\n\t"
    "s_add_u32 s82, s52, 31\n\t"
    "s_lshr_b32 s82, s82, 5\n\t"
    "s_add_u32 s83, s82, 7\n\t"
    "s_lshr_b32 s83, s83, 3\n\t"
    "s_sub_u32 s80, s27, s35\n\t"
    "s_lshl_b32 s54, s80, 3\n\t"
    "s_sub_u32 s55, s82, s54\n\t"
    "s_min_u32 s55, s55, 8\n\t"
    "s_lshl_b32 s53, s53, 12\n\t"
    "s_or_b32 s53, s53, s25\n\t"
    "s_cmp_lt_u32 s80, s83\n\t"
    ".p2align 5,,4\n\ts_cbranch_scc1 Lround_%=\n\t"
    "s_add_u32 s35, s35, s83\n\t"
    "s_add_u32 s34, s34, 1\n\t"
    "s_branch Lg44_%=\n\t"
    "Ldone_%=:\n\t"
    "s_waitcnt vmcnt(0) lgkmcnt(0)\n\t"
    : "+{v0}"(tid) : "s"(__builtin_amdgcn_kernarg_segment_ptr()), "s"(blockIdx.x)
    : "memory", "vcc", "scc",
      "v1","v2","v3","v4","v5","v6","v7","v8","v9","v10","v11","v12","v13","v14","v15","v16","v17","v18","v19","v20","v21","v22","v23","v24",
      "v25","v26","v27","v28","v29","v30","v31","v32","v33","v34","v35","v36","v37","v38","v39","v40","v41","v42","v43","v44","v45","v46","v47","v48",
      "v49","v50","v51","v52","v53","v54","v55","v56","v57","v58","v59","v60","v61","v62","v63","v64","v65","v66","v67","v68","v69","v70","v71","v72",
      "v73","v74","v75","v76","v77","v78","v79","v80","v81","v82","v83","v84","v85","v86","v87","v88","v89","v90","v91","v92","v93","v94","v95","v96",
      "v97","v98","v99","v100","v101","v102","v103","v104","v105","v106","v107","v108","v109","v110","v111","v112","v113","v114","v115","v116","v117","v118","v119","v120",
      "v121","v122","v123","v124","v125","v126","v127","v128","v129","v130","v131","v132","v133","v134","v135","v136","v137","v138","v139","v140","v141","v142","v143","v144",
      "v145","v146","v147","v148","v149","v150","v151","v152","v153","v154","v155","v156","v157","v158","v159","v160","v161","v162","v163","v164","v165","v166","v167","s3",
      "s4","s5","s6","s7","s8","s9","s10","s11","s12","s13","s14","s15","s16","s17","s18","s19","s20","s21","s22","s23","s24","s25","s26","s27",
      "s28","s29","s30","s31","s34","s35","s36","s37","s38","s39","s40","s41","s42","s43","s44","s45","s46","s47","s48","s49","s50","s51","s52","s53",
      "s54","s55","s56","s57","s58","s59","s60","s61","s62","s63","s64","s65","s66","s67","s68","s69","s70","s71","s72","s73","s74","s75","s76","s77",
      "s78","s79","s80","s81","s82","s83","s84","s85","s86","s87","s88","s89","s90","s91","s92","s93","s94","s95","s96","s97","s98","s99");
}

extern "C" void kernel_launch(void* const* d_in, const int* in_sizes, int n_in, void* d_out, int out_size, void* d_ws, size_t ws_size,
                              hipStream_t stream) {
  const int T = in_sizes[0] / (16 * 128), nseq = in_sizes[3] - 1;
  if (T <= 0 || nseq <= 0) return;
  (void)hipFuncSetAttribute((const void*)varlen_attn_kernel, hipFuncAttributeMaxDynamicSharedMemorySize, 163840);
  varlen_attn_kernel<<<256, 768, 163840, stream>>>(d_in[0], d_in[1], d_in[2], d_out, (const int*)d_in[3], d_ws, nseq,
                                                   (unsigned)T / (32u * 3u), (unsigned)T - 1u, 0u);
}
```
